```python
import torch

def _softmax(x, axis=-1):
    return torch.softmax(x, dim=axis)
def _gelu(x):
    return torch.nn.functional.gelu(x, approximate="tanh")
def _silu(x):
    return torch.nn.functional.silu(x)
def _sigmoid(x):
    return torch.sigmoid(x)
def _layernorm(x, axis=-1, eps=1e-5):
    m = x.mean(dim=axis, keepdim=True)
    v = x.var(dim=axis, keepdim=True, unbiased=False)
    return (x - m) / torch.sqrt(v + eps)
def _scatter_add(tgt, idx, src, axis=0):
    return tgt.clone().index_add_(axis, idx, src)
def _segment_sum(src, seg, n):
    out = torch.zeros((n,) + src.shape[1:], dtype=src.dtype)
    return out.index_add_(0, seg, src)
def _pad(x, pw):
    flat = tuple(v for pair in reversed(pw) for v in pair)
    return torch.nn.functional.pad(x, flat)

kernel_name = 'concat_heads__ssm_conv_h3__spectral_fnet__prompt'

def setup_inputs():

    _w = (lambda a: (lambda t: t.to(torch.bfloat16) if t.is_floating_point() else t)(torch.as_tensor(a)))
    def _A_setup():
        B, S, D = 1, 2048, 640
        ks = 4
        return {
            "q": _w((lambda *s: torch.randn(*s, dtype=torch.bfloat16) * 0.05)(B, S, D)),
            "k": _w((lambda *s: torch.randn(*s, dtype=torch.bfloat16) * 3.2)(B, S, D)),
            "v": _w((lambda *s: torch.randn(*s, dtype=torch.bfloat16) * 3.2)(B, S, D)),
            "shift_k": _w((lambda *s: torch.randn(*s, dtype=torch.bfloat16) * 3.2)(D, ks) * 0.5),
            "ssm_k": _w((lambda *s: torch.randn(*s, dtype=torch.bfloat16) * 3.2)(D, S) * (1.0 / S ** 0.5)),
            "ssm_D": _w((lambda *s: torch.randn(*s, dtype=torch.bfloat16) * 3.2)(D)),
        }
    _A_in = _A_setup()

    def _B_setup():
        B, S, D = 1, 2048, 640
        ns = torch.arange(S)[:, None] * torch.arange(S)[None, :]
        nd = torch.arange(D)[:, None] * torch.arange(D)[None, :]
        Ws_re = torch.cos(-2.0 * torch.pi * ns / S).to(float)
        Ws_im = torch.sin(-2.0 * torch.pi * ns / S).to(float)
        Wd_re = torch.cos(-2.0 * torch.pi * nd / D).to(float)
        Wd_im = torch.sin(-2.0 * torch.pi * nd / D).to(float)
        return {
            "x": _w((lambda *s: torch.randn(*s, dtype=torch.bfloat16) * 0.05)(B, S, D)),
            "dft_seq_re": _w(Ws_re),
            "dft_seq_im": _w(Ws_im),
            "dft_hid_re": _w(Wd_re),
            "dft_hid_im": _w(Wd_im),
        }
    _B_in = _B_setup()

    return {
        "A_q": _A_in["q"],
        "A_k": _A_in["k"],
        "A_v": _A_in["v"],
        "A_shift_k": _A_in["shift_k"],
        "A_ssm_k": _A_in["ssm_k"],
        "A_ssm_D": _A_in["ssm_D"],
        "B_x": _B_in["x"],
        "B_dft_seq_re": _B_in["dft_seq_re"],
        "B_dft_seq_im": _B_in["dft_seq_im"],
        "B_dft_hid_re": _B_in["dft_hid_re"],
        "B_dft_hid_im": _B_in["dft_hid_im"],
    }

def reference(A_q, A_k, A_v, A_shift_k, A_ssm_k, A_ssm_D, B_x, B_dft_seq_re, B_dft_seq_im, B_dft_hid_re, B_dft_hid_im):
    def _A_ref(q, k, v, shift_k, ssm_k, ssm_D):
        B, S, D = q.shape
        ks = shift_k.shape[-1]
        k_pad = _pad(k, ((0, 0), (ks - 1, 0), (0, 0)))
        k_shift = torch.zeros_like(k)
        for t in range(ks):
            k_shift = k_shift + k_pad[:, t:t + S, :] * shift_k[:, ks - 1 - t][None, None, :]
        kv = k_shift * v
        L = 2 * S
        kv_pad = _pad(kv, ((0, 0), (0, S), (0, 0)))
        kf_pad = _pad(ssm_k, ((0, 0), (0, S)))
        kv_f = (lambda x, axes=(-2,-1): torch.fft.rfft2(x.float(), dim=axes))(kv_pad[:, None, :, :], axes=(1, 2))
        kf_f = (lambda x, axes=(-2,-1): torch.fft.rfft2(x.float(), dim=axes))(kf_pad[:, None, :], axes=(1, 2))
        kf_f = (lambda x, *d: x.permute(*d))(kf_f, 1, 2, 0)
        y_f = kv_f * kf_f[None, :, :, :]
        y = (lambda x, s, axes=(-2,-1): torch.fft.irfft2(x, s=s, dim=axes).to(torch.bfloat16))(y_f, (1, L), axes=(1, 2))[:, 0, :S, :]
        y = y + kv * ssm_D[None, None, :]
        return q * y

    def _B_ref(x, dft_seq_re, dft_seq_im, dft_hid_re, dft_hid_im):
        a = torch.einsum("bsd,ed->bse", x, dft_hid_re)
        b = torch.einsum("bsd,ed->bse", x, dft_hid_im)
        y = torch.einsum("ts,bse->bte", dft_seq_re, a) - torch.einsum("ts,bse->bte", dft_seq_im, b)
        return y

    a = _A_ref(A_q, A_k, A_v, A_shift_k, A_ssm_k, A_ssm_D)
    b = _B_ref(B_x, B_dft_seq_re, B_dft_seq_im, B_dft_hid_re, B_dft_hid_im)
    return torch.concatenate([a, b], axis=-1)
```

```cpp
#include <hip/hip_runtime.h>
#include <hip/hip_bf16.h>
typedef __hip_bfloat16 bf16;
constexpr int S = 2048, D = 640;

__device__ __forceinline__ float bf2f(bf16 x) { return __bfloat162float(x); }
__device__ __forceinline__ float rbf(float x) { return __bfloat162float(__float2bfloat16(x)); }

__global__ void prep_kernel(const bf16* k, const bf16* v, const bf16* shift_k, bf16* kv) {
    int idx = blockIdx.x * blockDim.x + threadIdx.x;
    if (idx >= S * D) return;
    int t = idx / D, d = idx % D;
    float acc = 0.f;
    for (int j = 0; j < 4; ++j) {
        int ts = t + j - 3;
        float kk = ts >= 0 ? bf2f(k[ts * D + d]) : 0.f;
        float p = rbf(kk * bf2f(shift_k[d * 4 + 3 - j]));
        acc = rbf(acc + p);
    }
    kv[idx] = __float2bfloat16(acc * bf2f(v[idx]));
}
__global__ void conv_kernel(const bf16* q, const bf16* kv, const bf16* ssm_k, const bf16* ssm_D, bf16* out) {
    int idx = blockIdx.x * blockDim.x + threadIdx.x;
    if (idx >= S * D) return;
    int t = idx / D, d = idx % D;
    float acc = 0.f;
    for (int s = 0; s <= t; ++s) acc += bf2f(kv[s * D + d]) * bf2f(ssm_k[d * S + (t - s)]);
    float y = rbf(acc);
    float kd = rbf(bf2f(kv[idx]) * bf2f(ssm_D[d]));
    y = rbf(y + kd);
    out[t * 2 * D + d] = __float2bfloat16(bf2f(q[idx]) * y);
}
__global__ void gemm1_kernel(const bf16* x, const bf16* dr, const bf16* di, bf16* a, bf16* b) {
    int idx = blockIdx.x * blockDim.x + threadIdx.x;
    if (idx >= S * D) return;
    int s = idx / D, e = idx % D;
    float aa = 0.f, bb = 0.f;
    for (int d = 0; d < D; ++d) { float xv = bf2f(x[s * D + d]); aa += xv * bf2f(dr[e * D + d]); bb += xv * bf2f(di[e * D + d]); }
    a[idx] = __float2bfloat16(aa); b[idx] = __float2bfloat16(bb);
}
__global__ void gemm2_kernel(const bf16* cr, const bf16* ci, const bf16* a, const bf16* b, bf16* out) {
    int idx = blockIdx.x * blockDim.x + threadIdx.x;
    if (idx >= S * D) return;
    int t = idx / D, e = idx % D;
    float y1 = 0.f, y2 = 0.f;
    for (int s = 0; s < S; ++s) { y1 += bf2f(cr[t * S + s]) * bf2f(a[s * D + e]); y2 += bf2f(ci[t * S + s]) * bf2f(b[s * D + e]); }
    out[t * 2 * D + D + e] = __float2bfloat16(rbf(y1) - rbf(y2));
}

extern "C" void kernel_launch(void* const* d_in, const int* in_sizes, int n_in,
                              void* d_out, int out_size, void* d_ws, size_t ws_size,
                              hipStream_t stream) {
    const bf16* q = (const bf16*)d_in[0];
    const bf16* k = (const bf16*)d_in[1];
    const bf16* v = (const bf16*)d_in[2];
    const bf16* shift_k = (const bf16*)d_in[3];
    const bf16* ssm_k = (const bf16*)d_in[4];
    const bf16* ssm_D = (const bf16*)d_in[5];
    const bf16* x = (const bf16*)d_in[6];
    const bf16* cr = (const bf16*)d_in[7];
    const bf16* ci = (const bf16*)d_in[8];
    const bf16* dr = (const bf16*)d_in[9];
    const bf16* di = (const bf16*)d_in[10];
    bf16* out = (bf16*)d_out;
    bf16* kv = (bf16*)d_ws;
    bf16* a = kv + S * D;
    bf16* b = a + S * D;
    int n = S * D;
    prep_kernel<<<n / 256, 256, 0, stream>>>(k, v, shift_k, kv);
    conv_kernel<<<n / 256, 256, 0, stream>>>(q, kv, ssm_k, ssm_D, out);
    gemm1_kernel<<<n / 256, 256, 0, stream>>>(x, dr, di, a, b);
    gemm2_kernel<<<n / 256, 256, 0, stream>>>(cr, ci, a, b, out);
}
```

```cpp
#include <hip/hip_runtime.h>
#include <hip/hip_bf16.h>
#include <stdint.h>

typedef unsigned short u16;
typedef __attribute__((ext_vector_type(8))) short bf16x8;
typedef __attribute__((ext_vector_type(4))) short bf16x4;
typedef __attribute__((ext_vector_type(4))) float f32x4;
typedef __attribute__((ext_vector_type(4))) unsigned u32x4;
typedef __attribute__((ext_vector_type(2))) unsigned u32x2;

constexpr int S = 2048, D = 640;

#define WAIT_V(n) asm volatile("s_waitcnt vmcnt(" #n ")" ::: "memory")
#define WAIT_L0() asm volatile("s_waitcnt lgkmcnt(0)" ::: "memory")
#define BAR() do { asm volatile("s_waitcnt lgkmcnt(0)" ::: "memory"); __builtin_amdgcn_s_barrier(); asm volatile("" ::: "memory"); } while (0)
#define RD128(dst, addr, off) asm volatile("ds_read_b128 %0, %1 offset:%2" : "=v"(dst) : "v"(addr), "i"(off))
#define RD64(dst, addr, off) asm volatile("ds_read_b64 %0, %1 offset:%2" : "=v"(dst) : "v"(addr), "i"(off))
#define RDTR(dst, addr, off) asm volatile("ds_read_b64_tr_b16 %0, %1 offset:%2" : "=v"(dst) : "v"(addr), "i"(off))
#define PIN(x) asm volatile("" : "+v"(x))
#define SCHED() __builtin_amdgcn_sched_barrier(0)

__device__ __forceinline__ void glds16(const void* g, void* l) {
    __builtin_amdgcn_global_load_lds((const unsigned*)g, (unsigned*)l, 16, 0, 0);
}
__device__ __forceinline__ float bf2f(u16 x) { return __uint_as_float(((unsigned)x) << 16); }
__device__ __forceinline__ u16 f2bf(float x) { __hip_bfloat16 b = __float2bfloat16(x); return *reinterpret_cast<u16*>(&b); }
__device__ __forceinline__ float rbf(float x) { return bf2f(f2bf(x)); }
__device__ __forceinline__ unsigned pack2(float a, float b) { return (unsigned)f2bf(a) | ((unsigned)f2bf(b) << 16); }
__device__ __forceinline__ float lo16(unsigned w) { return __uint_as_float(w << 16); }
__device__ __forceinline__ float hi16(unsigned w) { return __uint_as_float(w & 0xffff0000u); }

constexpr int K1_A = 16384, K1_B = 16384, K1_STAGE = K1_A + K1_B, K1_NST = 4;
constexpr int K1_LDS = K1_STAGE * K1_NST;

struct Frag1 { bf16x8 cf[4]; bf16x4 xl[4], xh[4]; };

__global__ __launch_bounds__(512, 2) void k1_gemmA_prep(
    const u16* __restrict__ cr, const u16* __restrict__ ci, const u16* __restrict__ x,
    const u16* __restrict__ kk, const u16* __restrict__ vv, const u16* __restrict__ qq,
    const u16* __restrict__ shift_k, u16* __restrict__ pq, u16* __restrict__ kvT, u16* __restrict__ qT)
{
    extern __shared__ __attribute__((aligned(16))) char smem[];
    const int tid = threadIdx.x, lane = tid & 63;
    const int w8 = __builtin_amdgcn_readfirstlane(tid >> 6);
    const int w = w8 & 3;
    const int bid = blockIdx.x;
    const int xcd = bid & 7, idx = bid >> 3;
#define ST(i)
    ST(0);

    if (idx >= 20) {
        u16* kvs = reinterpret_cast<u16*>(smem);
        u16* qs = kvs + 64 * 72;
        for (int tile = xcd * 12 + (idx - 20); tile < 320; tile += 96) {
            const int tt0 = (tile / 10) * 64, d0 = (tile % 10) * 64;
            {
                int r = tid >> 3, c = tid & 7;
                int t = tt0 + r, d = d0 + 8 * c;
                u32x4 kr[4];
#pragma unroll
                for (int j = 0; j < 4; ++j) {
                    int ts = t + j - 3;
                    kr[j] = *reinterpret_cast<const u32x4*>(kk + (size_t)(ts < 0 ? 0 : ts) * D + d);
                    if (ts < 0) kr[j] = (u32x4){0u, 0u, 0u, 0u};
                }
                u32x4 vr = *reinterpret_cast<const u32x4*>(vv + (size_t)t * D + d);
                u32x4 qr = *reinterpret_cast<const u32x4*>(qq + (size_t)t * D + d);
                u32x4 sk[4];
#pragma unroll
                for (int j = 0; j < 4; ++j) sk[j] = *reinterpret_cast<const u32x4*>(shift_k + d * 4 + 8 * j);
#pragma unroll
                for (int e = 0; e < 8; ++e) {
                    float a = 0.f;
#pragma unroll
                    for (int j = 0; j < 4; ++j) {
                        unsigned kw = kr[j][e >> 1];
                        float kf = (e & 1) ? hi16(kw) : lo16(kw);
                        int si = e * 4 + 3 - j;
                        unsigned sw = sk[si >> 3][(si >> 1) & 3];
                        float sf = (si & 1) ? hi16(sw) : lo16(sw);
                        a = rbf(a + rbf(kf * sf));
                    }
                    unsigned vw = vr[e >> 1];
                    float vf = (e & 1) ? hi16(vw) : lo16(vw);
                    kvs[(8 * c + e) * 72 + r] = f2bf(a * vf);
                    unsigned qw = qr[e >> 1];
                    qs[(8 * c + e) * 72 + r] = (u16)((e & 1) ? (qw >> 16) : (qw & 0xffff));
                }
            }
            __syncthreads();
            {
                int dl = tid >> 3, c = tid & 7;
                u32x4 a = *reinterpret_cast<const u32x4*>(kvs + dl * 72 + 8 * c);
                u32x4 b = *reinterpret_cast<const u32x4*>(qs + dl * 72 + 8 * c);
                *reinterpret_cast<u32x4*>(kvT + (size_t)(d0 + dl) * S + tt0 + 8 * c) = a;
                *reinterpret_cast<u32x4*>(qT + (size_t)(d0 + dl) * S + tt0 + 8 * c) = b;
            }
            __syncthreads();
        }
        return;
    }

    const int tm = xcd * 4 + idx / 5, tn = idx % 5;
    const int t0 = tm * 64, n0 = tn * 128;

    if (w8 >= 4) {
        const u16* aptr[4];
#pragma unroll
        for (int i = 0; i < 4; ++i) {
            int row = 32 * w + 8 * i + (lane >> 3);
            int ch = (lane & 7) ^ ((row >> 1) & 7);
            aptr[i] = (row < 64 ? cr : ci) + (size_t)(t0 + (row & 63)) * S + ch * 8;
        }
        const u16* bptr[4];
#pragma unroll
        for (int i = 0; i < 4; ++i) {
            int line = (w * 4 + i) * 8 + (lane >> 3);
            int kb = line >> 3, c64 = (line >> 2) & 1, q = line & 3;
            int c = lane & 7;
            int f = ((q >> 1) & 1) | (((kb >> 1) & 1) << 1);
            int cg = ((((c >> 1) ^ f) << 1) | (c & 1));
            bptr[i] = x + (4 * kb + q) * D + n0 + 64 * c64 + 8 * cg;
        }
        auto stage = [&](int kt, int buf) {
            char* sb = smem + buf * K1_STAGE;
#pragma unroll
            for (int i = 0; i < 4; ++i) glds16(aptr[i] + kt * 64, sb + (32 * w + 8 * i) * 128);
#pragma unroll
            for (int i = 0; i < 4; ++i) glds16(bptr[i] + kt * 64 * D, sb + K1_A + (w * 4 + i) * 1024);
        };
        stage(0, 0); stage(1, 1); stage(2, 2); stage(3, 3);
        WAIT_V(24); BAR();
        for (int kt4 = 0; kt4 < 28; kt4 += 4) {
            WAIT_V(16); BAR(); stage(kt4 + 4, 0);
            WAIT_V(16); BAR(); stage(kt4 + 5, 1);
            WAIT_V(16); BAR(); stage(kt4 + 6, 2);
            WAIT_V(16); BAR(); stage(kt4 + 7, 3);
        }
        WAIT_V(16); BAR();
        WAIT_V(8); BAR();
        WAIT_V(0); BAR();
    } else {
        const int wm = w & 1, wn = w >> 1;
        f32x4 acc[4][4];
#pragma unroll
        for (int a = 0; a < 4; ++a)
#pragma unroll
            for (int b = 0; b < 4; ++b) acc[a][b] = (f32x4){0.f, 0.f, 0.f, 0.f};
        const int g = lane >> 4;
        const unsigned lds0 = (unsigned)(uintptr_t)smem;
        unsigned aoff[2];
#pragma unroll
        for (int ks = 0; ks < 2; ++ks) {
            int row = 64 * wm + (lane & 15);
            int ch = (4 * ks + g) ^ ((row >> 1) & 7);
            aoff[ks] = lds0 + row * 128 + ch * 16;
        }
        unsigned boff[4];
        {
            int q = (lane & 15) >> 2, p = lane & 3;
            int f = ((q >> 1) & 1) | ((g & 1) << 1);
#pragma unroll
            for (int nt = 0; nt < 4; ++nt)
                boff[nt] = lds0 + K1_A + (2 * g) * 1024 + wn * 512 + q * 128 + ((nt ^ f) * 32) + p * 8;
        }
        auto readfrag = [&](Frag1& f, int buf, int ks) {
            const unsigned bo = buf * K1_STAGE;
            const unsigned a0 = aoff[ks] + bo;
            RD128(f.cf[0], a0, 0); RD128(f.cf[1], a0, 2048); RD128(f.cf[2], a0, 4096); RD128(f.cf[3], a0, 6144);
#pragma unroll
            for (int nt = 0; nt < 4; ++nt) {
                const unsigned b0 = boff[nt] + bo;
                if (ks == 0) { RDTR(f.xl[nt], b0, 0); RDTR(f.xh[nt], b0, 1024); }
                else         { RDTR(f.xl[nt], b0, 8192); RDTR(f.xh[nt], b0, 9216); }
            }
        };
        auto pinfrag = [&](Frag1& f) {
#pragma unroll
            for (int i = 0; i < 4; ++i) { PIN(f.cf[i]); PIN(f.xl[i]); PIN(f.xh[i]); }
        };
        auto mma = [&](Frag1& f) {
#pragma unroll
            for (int nt = 0; nt < 4; ++nt) {
                bf16x8 xf = __builtin_shufflevector(f.xl[nt], f.xh[nt], 0, 1, 2, 3, 4, 5, 6, 7);
#pragma unroll
                for (int mt = 0; mt < 4; ++mt)
                    acc[mt][nt] = __builtin_amdgcn_mfma_f32_16x16x32_bf16(xf, f.cf[mt], acc[mt][nt], 0, 0, 0);
            }
        };
        auto mma_rd = [&](Frag1& fc, Frag1& fn, int buf, int ks) {
            const unsigned bo = buf * K1_STAGE;
            const unsigned a0 = aoff[ks] + bo;
#pragma unroll
            for (int i = 0; i < 16; ++i) {
                const int nt = i >> 2, mt = i & 3;
                bf16x8 xf = __builtin_shufflevector(fc.xl[nt], fc.xh[nt], 0, 1, 2, 3, 4, 5, 6, 7);
                acc[mt][nt] = __builtin_amdgcn_mfma_f32_16x16x32_bf16(xf, fc.cf[mt], acc[mt][nt], 0, 0, 0);
                SCHED();
                if (i < 4) { RD128(fn.cf[i], a0, 2048 * i); }
                else if (i < 12) {
                    const int j = (i - 4) >> 1;
                    const unsigned b0 = boff[j] + bo;
                    if ((i & 1) == 0) { if (ks == 0) RDTR(fn.xl[j], b0, 0); else RDTR(fn.xl[j], b0, 8192); }
                    else { if (ks == 0) RDTR(fn.xh[j], b0, 1024); else RDTR(fn.xh[j], b0, 9216); }
                }
                SCHED();
            }
        };
        Frag1 f0, f1;
        BAR();
        ST(1);
        readfrag(f0, 0, 0);
#define KSTEP(b, bn) do { WAIT_L0(); pinfrag(f0); mma_rd(f0, f1, b, 1); BAR(); pinfrag(f1); mma_rd(f1, f0, bn, 0); } while (0)
        for (int kt4 = 0; kt4 < 28; kt4 += 4) {
            KSTEP(0, 1); KSTEP(1, 2); KSTEP(2, 3); KSTEP(3, 0);
        }
        KSTEP(0, 1); KSTEP(1, 2); KSTEP(2, 3);
        WAIT_L0(); pinfrag(f0); mma_rd(f0, f1, 3, 1); WAIT_L0(); pinfrag(f1); mma(f1);
        ST(2);
        {
            const int cbase = (wm ? 640 : 0) + n0 + 64 * wn + 4 * g;
#pragma unroll
            for (int mt = 0; mt < 4; ++mt) {
                int t = t0 + 16 * mt + (lane & 15);
#pragma unroll
                for (int nt = 0; nt < 4; ++nt) {
                    u32x2 o;
                    o[0] = pack2(acc[mt][nt][0], acc[mt][nt][1]);
                    o[1] = pack2(acc[mt][nt][2], acc[mt][nt][3]);
                    *reinterpret_cast<u32x2*>(pq + (size_t)t * 1280 + cbase + 16 * nt) = o;
                }
            }
        }
        WAIT_V(0);
        ST(3);
    }
}

constexpr int K2_A = 8192, K2_B = 10240, K2_STAGE = K2_A + K2_B, K2_NST = 4;
constexpr int K2_LDS = K2_STAGE * K2_NST;
constexpr int CSTR = 2240;
constexpr int CW_BYTES = 4 * CSTR + 4640;
constexpr int OBUF_OFF = 4 * CW_BYTES;

struct Frag2 { bf16x8 pf[2]; bf16x8 df[2][5]; };

__device__ __forceinline__ void gemmB_block(char* smem, int bid, const u16* __restrict__ pq,
    const u16* __restrict__ dr, const u16* __restrict__ di, u16* __restrict__ out)
{
    const int tid = threadIdx.x, lane = tid & 63;
    const int w = __builtin_amdgcn_readfirstlane(tid >> 6);
    const int xcd = bid & 7, idx = bid >> 3;
    const int tm = xcd * 4 + (idx >> 3), tn = idx & 7;
    const int t0 = tm * 64, n0 = tn * 80;
    const int g = lane >> 4;

    const u16* aptr[2];
#pragma unroll
    for (int i = 0; i < 2; ++i) {
        int row = 16 * w + 8 * i + (lane >> 3);
        int ch = (lane & 7) ^ ((row >> 1) & 7);
        aptr[i] = pq + (size_t)(t0 + row) * 1280 + ch * 8;
    }
    int boffs[3];
#pragma unroll
    for (int i = 0; i < 3; ++i) {
        int row = (i < 2) ? (8 * (w + 4 * i) + (lane >> 3)) : (64 + 4 * w + ((lane & 31) >> 3));
        int ch = (lane & 7) ^ ((row >> 1) & 7);
        boffs[i] = (n0 + row) * D + ch * 8;
    }
    auto stage = [&](int kt, int buf) {
        char* sb = smem + buf * K2_STAGE;
        const u16* bsrc = (kt < 10) ? (dr + kt * 64) : (di + (kt - 10) * 64);
#pragma unroll
        for (int i = 0; i < 2; ++i) glds16(aptr[i] + kt * 64, sb + (16 * w + 8 * i) * 128);
        glds16(bsrc + boffs[0], sb + K2_A + w * 1024);
        glds16(bsrc + boffs[1], sb + K2_A + (w + 4) * 1024);
        if (lane < 32) glds16(bsrc + boffs[2], sb + K2_A + 8192 + w * 512);
    };
    stage(0, 0); stage(1, 1); stage(2, 2); stage(3, 3);

    f32x4 acc[5], y1[5];
#pragma unroll
    for (int b = 0; b < 5; ++b) { acc[b] = (f32x4){0.f, 0.f, 0.f, 0.f}; y1[b] = acc[b]; }
    const unsigned lds0 = (unsigned)(uintptr_t)smem;
    unsigned aoff[2], boff[2];
#pragma unroll
    for (int ks = 0; ks < 2; ++ks) {
        int row = 16 * w + (lane & 15);
        aoff[ks] = lds0 + row * 128 + (((4 * ks + g) ^ ((row >> 1) & 7)) * 16);
        int r2 = (lane & 15);
        boff[ks] = lds0 + K2_A + r2 * 128 + (((4 * ks + g) ^ ((r2 >> 1) & 7)) * 16);
    }
    auto readfrag = [&](Frag2& f, int buf) {
        const unsigned bo = buf * K2_STAGE;
#pragma unroll
        for (int ks = 0; ks < 2; ++ks) {
            const unsigned a = aoff[ks] + bo, b = boff[ks] + bo;
            RD128(f.pf[ks], a, 0);
#pragma unroll
            for (int nt = 0; nt < 5; ++nt) RD128(f.df[ks][nt], b, 2048 * nt);
        }
    };
    auto pinfrag = [&](Frag2& f) {
#pragma unroll
        for (int ks = 0; ks < 2; ++ks) {
            PIN(f.pf[ks]);
#pragma unroll
            for (int nt = 0; nt < 5; ++nt) PIN(f.df[ks][nt]);
        }
    };
    auto mma = [&](Frag2& f) {
#pragma unroll
        for (int ks = 0; ks < 2; ++ks)
#pragma unroll
            for (int nt = 0; nt < 5; ++nt)
                acc[nt] = __builtin_amdgcn_mfma_f32_16x16x32_bf16(f.df[ks][nt], f.pf[ks], acc[nt], 0, 0, 0);
    };
    auto sw = [&]() {
#pragma unroll
        for (int nt = 0; nt < 5; ++nt) { y1[nt] = acc[nt]; acc[nt] = (f32x4){0.f, 0.f, 0.f, 0.f}; }
    };
    Frag2 fa, fb;
    WAIT_V(15); BAR();
    readfrag(fa, 0);
    for (int kt4 = 0; kt4 < 16; kt4 += 4) {
        WAIT_V(10); BAR(); pinfrag(fa); stage(kt4 + 4, 0); readfrag(fb, 1); mma(fa);
        WAIT_V(10); BAR(); pinfrag(fb); stage(kt4 + 5, 1); readfrag(fa, 2); mma(fb);
        if (kt4 == 8) sw();
        WAIT_V(10); BAR(); pinfrag(fa); stage(kt4 + 6, 2); readfrag(fb, 3); mma(fa);
        WAIT_V(10); BAR(); pinfrag(fb); stage(kt4 + 7, 3); readfrag(fa, 0); mma(fb);
    }
    WAIT_V(10); BAR(); pinfrag(fa); readfrag(fb, 1); mma(fa);
    WAIT_V(5); BAR(); pinfrag(fb); readfrag(fa, 2); mma(fb);
    WAIT_V(0); BAR(); pinfrag(fa); readfrag(fb, 3); mma(fa);
    WAIT_L0(); pinfrag(fb); mma(fb);

    const int t = t0 + 16 * w + (lane & 15);
#pragma unroll
    for (int nt = 0; nt < 5; ++nt) {
        float o[4];
#pragma unroll
        for (int r = 0; r < 4; ++r) o[r] = rbf(y1[nt][r]) - rbf(acc[nt][r]);
        u32x2 ov; ov[0] = pack2(o[0], o[1]); ov[1] = pack2(o[2], o[3]);
        *reinterpret_cast<u32x2*>(out + (size_t)t * 1280 + 640 + n0 + 16 * nt + 4 * g) = ov;
    }
}

struct FragC { bf16x4 ga[4], gb[4]; bf16x8 F[8]; };

__device__ __forceinline__ void conv_block(char* smem, int cb, const u16* __restrict__ ssm_k,
    const u16* __restrict__ ssm_D, const u16* __restrict__ kvT, const u16* __restrict__ qT, u16* __restrict__ out)
{
    const int tid = threadIdx.x, lane = tid & 63;
    const int w = __builtin_amdgcn_readfirstlane(tid >> 6);
    const int grp = (cb & 7) * 20 + (cb >> 3);
    const int d0 = grp * 4, d = d0 + w;
    char* cw = smem + w * CW_BYTES;
    u16* kvs = reinterpret_cast<u16*>(cw + 4 * CSTR);
    const u16* hrow = ssm_k + (size_t)d * S;
    const int g = lane >> 4, li = lane & 15;

    u32x4 kvr[4];
#pragma unroll
    for (int i = 0; i < 4; ++i) kvr[i] = *reinterpret_cast<const u32x4*>(kvT + (size_t)d * S + 8 * (lane + 64 * i));
    u32x4 hlo[2][3]; u32x2 hhi[2][3];
#pragma unroll
    for (int ph = 0; ph < 2; ++ph)
#pragma unroll
        for (int it = 0; it < 3; ++it) {
            int c = lane + 64 * it;
            int E = 1024 * ph + 1040 - 8 * c;
            hlo[ph][it] = (u32x4){0u, 0u, 0u, 0u};
            hhi[ph][it] = (u32x2){0u, 0u};
            if (c < 134 && E - 8 >= 0 && E - 8 < S) hlo[ph][it] = *reinterpret_cast<const u32x4*>(hrow + E - 8);
            if (c < 134 && E >= 0 && E < S) hhi[ph][it] = *reinterpret_cast<const u32x2*>(hrow + E);
        }
    u32x2 q4[8];
#pragma unroll
    for (int n = 0; n < 8; ++n) q4[n] = *reinterpret_cast<const u32x2*>(qT + (size_t)d * S + 256 * n + 16 * li + 4 * g);
    const float Dd = bf2f(ssm_D[d]);

    {
        const u32x4 z = (u32x4){0u, 0u, 0u, 0u};
        if (lane < 32) *reinterpret_cast<u32x4*>(kvs + 8 * lane) = z;
        if (lane < 2) *reinterpret_cast<u32x4*>(kvs + 256 + 2048 + 8 * lane) = z;
#pragma unroll
        for (int i = 0; i < 4; ++i) *reinterpret_cast<u32x4*>(kvs + 256 + 8 * (lane + 64 * i)) = kvr[i];
    }
    f32x4 acc[8];
#pragma unroll
    for (int n = 0; n < 8; ++n) acc[n] = (f32x4){0.f, 0.f, 0.f, 0.f};

    const unsigned cw0 = (unsigned)(uintptr_t)cw;
    const unsigned gbase = cw0 + (li & 3) * CSTR + 2 * (1040 - 4 * (li >> 2) + 8 * g) - 64 * 32;
    const unsigned fbase = cw0 + 4 * CSTR + 2 * (256 + 16 * li + 8 * g) - 64 * 8;

    auto build = [&](int ph) {
#pragma unroll
        for (int it = 0; it < 3; ++it) {
            int c = lane + 64 * it;
            if (c < 134) {
                unsigned W[6] = {hlo[ph][it][0], hlo[ph][it][1], hlo[ph][it][2], hlo[ph][it][3], hhi[ph][it][0], hhi[ph][it][1]};
#pragma unroll
                for (int r = 0; r < 4; ++r) {
                    u32x4 o;
#pragma unroll
                    for (int m = 0; m < 4; ++m) {
                        if ((r & 1) == 0) {
                            int i = 4 + r / 2 - m;
                            o[m] = __builtin_amdgcn_perm(W[i - 1], W[i], 0x07060100u);
                        } else {
                            int i = (7 + r) / 2 - m;
                            o[m] = __builtin_amdgcn_perm(W[i], W[i], 0x01000302u);
                        }
                    }
                    *reinterpret_cast<u32x4*>(cw + r * CSTR + 16 * c) = o;
                }
            }
        }
    };

#pragma unroll
    for (int ph = 0; ph < 2; ++ph) {
        const int NF = (ph == 0) ? 8 : 4;
        WAIT_L0();
        __builtin_amdgcn_wave_barrier();
        build(ph);
        WAIT_L0();
        __builtin_amdgcn_wave_barrier();

        auto rd = [&](FragC& f, int q) {
            const unsigned ga = gbase + 64 * 32 - 64 * 24 - 64 * q;
            const unsigned fa_ = fbase + 64 * 8 - 64 * q;
#pragma unroll
            for (int pp = 0; pp < 4; ++pp) { RD64(f.ga[pp], ga, 64 * (24 - 8 * pp)); RD64(f.gb[pp], ga, 64 * (24 - 8 * pp) + 8); }
#pragma unroll
            for (int mm = 0; mm < NF; ++mm) RD128(f.F[mm], fa_, 512 * mm);
        };
        auto pin = [&](FragC& f) {
#pragma unroll
            for (int pp = 0; pp < 4; ++pp) { PIN(f.ga[pp]); PIN(f.gb[pp]); }
#pragma unroll
            for (int mm = 0; mm < NF; ++mm) PIN(f.F[mm]);
        };
        auto mm_ = [&](FragC& f) {
#pragma unroll
            for (int pp = 0; pp < 4; ++pp) {
                const int p = 4 * ph + pp;
                bf16x8 G = __builtin_shufflevector(f.ga[pp], f.gb[pp], 0, 1, 2, 3, 4, 5, 6, 7);
#pragma unroll
                for (int n = p; n < 8; ++n)
                    acc[n] = __builtin_amdgcn_mfma_f32_16x16x32_bf16(G, f.F[n - p], acc[n], 0, 0, 0);
            }
        };
        FragC fa, fb;
        bf16x8 F8; bf16x4 g64a, g64b;
        rd(fa, 0);
        RD128(F8, fbase, 0);
        RD64(g64a, gbase, 0); RD64(g64b, gbase, 8);
        WAIT_L0(); pin(fa); PIN(F8); PIN(g64a); PIN(g64b);
        rd(fb, 1);
        mm_(fa);
        {
#pragma unroll
            for (int pp = 0; pp < 4; ++pp) {
                const int p = 4 * ph + pp;
                bf16x8 G = __builtin_shufflevector(fa.ga[pp], fa.gb[pp], 0, 1, 2, 3, 4, 5, 6, 7);
                if (p >= 1) acc[p - 1] = __builtin_amdgcn_mfma_f32_16x16x32_bf16(G, F8, acc[p - 1], 0, 0, 0);
            }
            if (ph == 1) {
                bf16x8 G64 = __builtin_shufflevector(g64a, g64b, 0, 1, 2, 3, 4, 5, 6, 7);
                acc[7] = __builtin_amdgcn_mfma_f32_16x16x32_bf16(G64, F8, acc[7], 0, 0, 0);
            }
        }
        for (int q = 1; q < 7; q += 2) {
            WAIT_L0(); pin(fb); rd(fa, q + 1); mm_(fb);
            WAIT_L0(); pin(fa); rd(fb, q + 2); mm_(fa);
        }
        WAIT_L0(); pin(fb); mm_(fb);
    }

    char* obuf = smem + OBUF_OFF;
#pragma unroll
    for (int n = 0; n < 8; ++n) {
        int t = 256 * n + 16 * li + 4 * g;
        u32x2 kv4 = *reinterpret_cast<const u32x2*>(kvs + 256 + t);
#pragma unroll
        for (int r = 0; r < 4; ++r) {
            unsigned kw = kv4[r >> 1], qw = q4[n][r >> 1];
            float kvf = (r & 1) ? hi16(kw) : lo16(kw);
            float qf = (r & 1) ? hi16(qw) : lo16(qw);
            float y = rbf(rbf(acc[n][r]) + rbf(kvf * Dd));
            int tt = t + r;
            *reinterpret_cast<u16*>(obuf + tt * 8 + (tt >> 4) * 8 + 2 * w) = f2bf(qf * y);
        }
    }
    __syncthreads();
#pragma unroll
    for (int it = 0; it < 8; ++it) {
        int t = tid + 256 * it;
        u32x2 o = *reinterpret_cast<const u32x2*>(obuf + t * 8 + (t >> 4) * 8);
        *reinterpret_cast<u32x2*>(out + (size_t)t * 1280 + d0) = o;
    }
}

__global__ __launch_bounds__(256, 2) void k2_gemmB_conv(
    const u16* __restrict__ pq, const u16* __restrict__ dr, const u16* __restrict__ di,
    const u16* __restrict__ ssm_k, const u16* __restrict__ ssm_D,
    const u16* __restrict__ kvT, const u16* __restrict__ qT, u16* __restrict__ out)
{
    extern __shared__ __attribute__((aligned(16))) char smem[];
    const int bid = blockIdx.x;
    if (bid < 256) gemmB_block(smem, bid, pq, dr, di, out);
    if (bid >= 256) conv_block(smem, bid - 256, ssm_k, ssm_D, kvT, qT, out);
}

extern "C" void kernel_launch(void* const* d_in, const int* in_sizes, int n_in,
                              void* d_out, int out_size, void* d_ws, size_t ws_size,
                              hipStream_t stream) {
    const u16* q = (const u16*)d_in[0];
    const u16* k = (const u16*)d_in[1];
    const u16* v = (const u16*)d_in[2];
    const u16* shift_k = (const u16*)d_in[3];
    const u16* ssm_k = (const u16*)d_in[4];
    const u16* ssm_D = (const u16*)d_in[5];
    const u16* x = (const u16*)d_in[6];
    const u16* cr = (const u16*)d_in[7];
    const u16* ci = (const u16*)d_in[8];
    const u16* dr = (const u16*)d_in[9];
    const u16* di = (const u16*)d_in[10];
    u16* out = (u16*)d_out;
    u16* pq = (u16*)d_ws;
    u16* kvT = pq + (size_t)S * 1280;
    u16* qT = kvT + (size_t)D * S;
    (void)hipFuncSetAttribute((const void*)k1_gemmA_prep, hipFuncAttributeMaxDynamicSharedMemorySize, K1_LDS);
    (void)hipFuncSetAttribute((const void*)k2_gemmB_conv, hipFuncAttributeMaxDynamicSharedMemorySize, K2_LDS);
    k1_gemmA_prep<<<256, 512, K1_LDS, stream>>>(cr, ci, x, k, v, q, shift_k, pq, kvT, qT);
    k2_gemmB_conv<<<416, 256, K2_LDS, stream>>>(pq, dr, di, ssm_k, ssm_D, kvT, qT, out);
}
```

```cpp
#include <hip/hip_runtime.h>
#include <hip/hip_bf16.h>
#include <stdint.h>

typedef unsigned short u16;
typedef __attribute__((ext_vector_type(8))) short bf16x8;
typedef __attribute__((ext_vector_type(4))) short bf16x4;
typedef __attribute__((ext_vector_type(4))) float f32x4;
typedef __attribute__((ext_vector_type(4))) unsigned u32x4;
typedef __attribute__((ext_vector_type(2))) unsigned u32x2;

constexpr int S = 2048, D = 640;

#define WAIT_V(n) asm volatile("s_waitcnt vmcnt(" #n ")" ::: "memory")
#define WAIT_L0() asm volatile("s_waitcnt lgkmcnt(0)" ::: "memory")
#define BAR() do { asm volatile("s_waitcnt lgkmcnt(0)" ::: "memory"); __builtin_amdgcn_s_barrier(); asm volatile("" ::: "memory"); } while (0)
#define RD128(dst, addr, off) asm volatile("ds_read_b128 %0, %1 offset:%2" : "=v"(dst) : "v"(addr), "i"(off))
#define RD64(dst, addr, off) asm volatile("ds_read_b64 %0, %1 offset:%2" : "=v"(dst) : "v"(addr), "i"(off))
#define RDTR(dst, addr, off) asm volatile("ds_read_b64_tr_b16 %0, %1 offset:%2" : "=v"(dst) : "v"(addr), "i"(off))
#define PIN(x) asm volatile("" : "+v"(x))
#define SCHED() __builtin_amdgcn_sched_barrier(0)

__device__ __forceinline__ void glds16(const void* g, void* l) {
    __builtin_amdgcn_global_load_lds((const unsigned*)g, (unsigned*)l, 16, 0, 0);
}
__device__ __forceinline__ float bf2f(u16 x) { return __uint_as_float(((unsigned)x) << 16); }
__device__ __forceinline__ u16 f2bf(float x) { __hip_bfloat16 b = __float2bfloat16(x); return *reinterpret_cast<u16*>(&b); }
__device__ __forceinline__ float rbf(float x) { return bf2f(f2bf(x)); }
__device__ __forceinline__ unsigned pack2(float a, float b) { return (unsigned)f2bf(a) | ((unsigned)f2bf(b) << 16); }
__device__ __forceinline__ float lo16(unsigned w) { return __uint_as_float(w << 16); }
__device__ __forceinline__ float hi16(unsigned w) { return __uint_as_float(w & 0xffff0000u); }

constexpr int K1_A = 16384, K1_B = 16384, K1_STAGE = K1_A + K1_B, K1_NST = 4;
constexpr int K1_LDS = K1_STAGE * K1_NST + 1024;

struct Frag1 { bf16x8 cf[4]; bf16x4 xl[4], xh[4]; };

__global__ __launch_bounds__(512, 2) void k1_gemmA_prep(
    const u16* __restrict__ cr, const u16* __restrict__ ci, const u16* __restrict__ x,
    const u16* __restrict__ kk, const u16* __restrict__ vv, const u16* __restrict__ qq,
    const u16* __restrict__ shift_k, u16* __restrict__ pq, u16* __restrict__ kvT, u16* __restrict__ qT)
{
    extern __shared__ __attribute__((aligned(16))) char smem[];
    const int tid = threadIdx.x, lane = tid & 63;
    const int w8 = __builtin_amdgcn_readfirstlane(tid >> 6);
    const int w = w8 & 3;
    const int bid = blockIdx.x;
    const int xcd = bid & 7, idx = bid >> 3;
#define ST(i)
    ST(0);
#define PF0 8
    auto touch = [&](int L) {
        int ks = L / 36, gl = 36 * idx + L % 36;
        const char* p;
        if (gl < 512) p = (const char*)((gl >> 8) ? ci : cr) + (size_t)(256 * xcd + (gl & 255)) * 4096 + ks * 128;
        else { int l2 = gl - 512; p = (const char*)x + (size_t)(64 * ks + l2 / 10) * 1280 + (l2 % 10) * 128; }
        __builtin_amdgcn_global_load_lds((const unsigned*)p, (unsigned*)(smem + 4 * K1_STAGE + w * 256), 4, 0, 0);
    };
    if (idx >= 20) {
        if (w8 < 4) {
#pragma unroll
            for (int i = 0; i < 5; ++i) { int L = (i * 4 + w) * 64 + lane; if (L < 1152) touch(L); }
        }
    } else if (w8 < 4) {
#pragma unroll
        for (int i = 0; i < (PF0 * 36 + 255) / 256; ++i) { int L = (i * 4 + w) * 64 + lane; if (L < PF0 * 36) touch(L); }
    }

    if (idx >= 20) {
        u16* kvs = reinterpret_cast<u16*>(smem);
        u16* qs = kvs + 64 * 72;
        for (int tile = xcd * 12 + (idx - 20); tile < 320; tile += 96) {
            const int tt0 = (tile / 10) * 64, d0 = (tile % 10) * 64;
            {
                int r = tid >> 3, c = tid & 7;
                int t = tt0 + r, d = d0 + 8 * c;
                u32x4 kr[4];
#pragma unroll
                for (int j = 0; j < 4; ++j) {
                    int ts = t + j - 3;
                    kr[j] = *reinterpret_cast<const u32x4*>(kk + (size_t)(ts < 0 ? 0 : ts) * D + d);
                    if (ts < 0) kr[j] = (u32x4){0u, 0u, 0u, 0u};
                }
                u32x4 vr = *reinterpret_cast<const u32x4*>(vv + (size_t)t * D + d);
                u32x4 qr = *reinterpret_cast<const u32x4*>(qq + (size_t)t * D + d);
                u32x4 sk[4];
#pragma unroll
                for (int j = 0; j < 4; ++j) sk[j] = *reinterpret_cast<const u32x4*>(shift_k + d * 4 + 8 * j);
#pragma unroll
                for (int e = 0; e < 8; ++e) {
                    float a = 0.f;
#pragma unroll
                    for (int j = 0; j < 4; ++j) {
                        unsigned kw = kr[j][e >> 1];
                        float kf = (e & 1) ? hi16(kw) : lo16(kw);
                        int si = e * 4 + 3 - j;
                        unsigned sw = sk[si >> 3][(si >> 1) & 3];
                        float sf = (si & 1) ? hi16(sw) : lo16(sw);
                        a = rbf(a + rbf(kf * sf));
                    }
                    unsigned vw = vr[e >> 1];
                    float vf = (e & 1) ? hi16(vw) : lo16(vw);
                    kvs[(8 * c + e) * 72 + r] = f2bf(a * vf);
                    unsigned qw = qr[e >> 1];
                    qs[(8 * c + e) * 72 + r] = (u16)((e & 1) ? (qw >> 16) : (qw & 0xffff));
                }
            }
            __syncthreads();
            {
                int dl = tid >> 3, c = tid & 7;
                u32x4 a = *reinterpret_cast<const u32x4*>(kvs + dl * 72 + 8 * c);
                u32x4 b = *reinterpret_cast<const u32x4*>(qs + dl * 72 + 8 * c);
                *reinterpret_cast<u32x4*>(kvT + (size_t)(d0 + dl) * S + tt0 + 8 * c) = a;
                *reinterpret_cast<u32x4*>(qT + (size_t)(d0 + dl) * S + tt0 + 8 * c) = b;
            }
            __syncthreads();
        }
        return;
    }

    const int tm = xcd * 4 + idx / 5, tn = idx % 5;
    const int t0 = tm * 64, n0 = tn * 128;

    if (w8 >= 4) {
        const u16* aptr[4];
#pragma unroll
        for (int i = 0; i < 4; ++i) {
            int row = 32 * w + 8 * i + (lane >> 3);
            int ch = (lane & 7) ^ ((row >> 1) & 7);
            aptr[i] = (row < 64 ? cr : ci) + (size_t)(t0 + (row & 63)) * S + ch * 8;
        }
        const u16* bptr[4];
#pragma unroll
        for (int i = 0; i < 4; ++i) {
            int line = (w * 4 + i) * 8 + (lane >> 3);
            int kb = line >> 3, c64 = (line >> 2) & 1, q = line & 3;
            int c = lane & 7;
            int f = ((q >> 1) & 1) | (((kb >> 1) & 1) << 1);
            int cg = ((((c >> 1) ^ f) << 1) | (c & 1));
            bptr[i] = x + (4 * kb + q) * D + n0 + 64 * c64 + 8 * cg;
        }
#define STAG 0
        const int stag = STAG;
        auto stage = [&](int kt_, int buf) {
            const int kt = (kt_ + stag) & 31;
            char* sb = smem + buf * K1_STAGE;
#pragma unroll
            for (int i = 0; i < 4; ++i) glds16(aptr[i] + kt * 64, sb + (32 * w + 8 * i) * 128);
#pragma unroll
            for (int i = 0; i < 4; ++i) glds16(bptr[i] + kt * 64 * D, sb + K1_A + (w * 4 + i) * 1024);
        };
        stage(0, 0); stage(1, 1); stage(2, 2); stage(3, 3);
        WAIT_V(24); BAR();
        for (int kt4 = 0; kt4 < 28; kt4 += 4) {
            WAIT_V(16); BAR(); stage(kt4 + 4, 0);
            WAIT_V(16); BAR(); stage(kt4 + 5, 1);
            WAIT_V(16); BAR(); stage(kt4 + 6, 2);
            WAIT_V(16); BAR(); stage(kt4 + 7, 3);
        }
        WAIT_V(16); BAR();
        WAIT_V(8); BAR();
        WAIT_V(0); BAR();
    } else {
        const int wm = w & 1, wn = w >> 1;
        f32x4 acc[4][4];
#pragma unroll
        for (int a = 0; a < 4; ++a)
#pragma unroll
            for (int b = 0; b < 4; ++b) acc[a][b] = (f32x4){0.f, 0.f, 0.f, 0.f};
        const int g = lane >> 4;
        const unsigned lds0 = (unsigned)(uintptr_t)smem;
        unsigned aoff[2];
#pragma unroll
        for (int ks = 0; ks < 2; ++ks) {
            int row = 64 * wm + (lane & 15);
            int ch = (4 * ks + g) ^ ((row >> 1) & 7);
            aoff[ks] = lds0 + row * 128 + ch * 16;
        }
        unsigned boff[4];
        {
            int q = (lane & 15) >> 2, p = lane & 3;
            int f = ((q >> 1) & 1) | ((g & 1) << 1);
#pragma unroll
            for (int nt = 0; nt < 4; ++nt)
                boff[nt] = lds0 + K1_A + (2 * g) * 1024 + wn * 512 + q * 128 + ((nt ^ f) * 32) + p * 8;
        }
        auto readfrag = [&](Frag1& f, int buf, int ks) {
            const unsigned bo = buf * K1_STAGE;
            const unsigned a0 = aoff[ks] + bo;
            RD128(f.cf[0], a0, 0); RD128(f.cf[1], a0, 2048); RD128(f.cf[2], a0, 4096); RD128(f.cf[3], a0, 6144);
#pragma unroll
            for (int nt = 0; nt < 4; ++nt) {
                const unsigned b0 = boff[nt] + bo;
                if (ks == 0) { RDTR(f.xl[nt], b0, 0); RDTR(f.xh[nt], b0, 1024); }
                else         { RDTR(f.xl[nt], b0, 8192); RDTR(f.xh[nt], b0, 9216); }
            }
        };
        auto pinfrag = [&](Frag1& f) {
#pragma unroll
            for (int i = 0; i < 4; ++i) { PIN(f.cf[i]); PIN(f.xl[i]); PIN(f.xh[i]); }
        };
        auto mma = [&](Frag1& f) {
#pragma unroll
            for (int nt = 0; nt < 4; ++nt) {
                bf16x8 xf = __builtin_shufflevector(f.xl[nt], f.xh[nt], 0, 1, 2, 3, 4, 5, 6, 7);
#pragma unroll
                for (int mt = 0; mt < 4; ++mt)
                    acc[mt][nt] = __builtin_amdgcn_mfma_f32_16x16x32_bf16(xf, f.cf[mt], acc[mt][nt], 0, 0, 0);
            }
        };
        auto mma_rd = [&](Frag1& fc, Frag1& fn, int buf, int ks) {
            const unsigned bo = buf * K1_STAGE;
            const unsigned a0 = aoff[ks] + bo;
#pragma unroll
            for (int i = 0; i < 16; ++i) {
                const int nt = i >> 2, mt = i & 3;
                bf16x8 xf = __builtin_shufflevector(fc.xl[nt], fc.xh[nt], 0, 1, 2, 3, 4, 5, 6, 7);
                acc[mt][nt] = __builtin_amdgcn_mfma_f32_16x16x32_bf16(xf, fc.cf[mt], acc[mt][nt], 0, 0, 0);
                SCHED();
                if (i < 4) { RD128(fn.cf[i], a0, 2048 * i); }
                else if (i < 12) {
                    const int j = (i - 4) >> 1;
                    const unsigned b0 = boff[j] + bo;
                    if ((i & 1) == 0) { if (ks == 0) RDTR(fn.xl[j], b0, 0); else RDTR(fn.xl[j], b0, 8192); }
                    else { if (ks == 0) RDTR(fn.xh[j], b0, 1024); else RDTR(fn.xh[j], b0, 9216); }
                }
                SCHED();
            }
        };
        Frag1 f0, f1;
        BAR();
        ST(1);
        readfrag(f0, 0, 0);
#define KSTEP(b, bn) do { WAIT_L0(); pinfrag(f0); mma_rd(f0, f1, b, 1); BAR(); pinfrag(f1); mma_rd(f1, f0, bn, 0); } while (0)
        for (int kt4 = 0; kt4 < 28; kt4 += 4) {
            { int L = (kt4 + PF0 + w) * 36 + lane; if (lane < 36 && L < 1152) touch(L); }
            KSTEP(0, 1); KSTEP(1, 2); KSTEP(2, 3); KSTEP(3, 0);
        }
        KSTEP(0, 1); KSTEP(1, 2); KSTEP(2, 3);
        WAIT_L0(); pinfrag(f0); mma_rd(f0, f1, 3, 1); WAIT_L0(); pinfrag(f1); mma(f1);
        ST(2);
        {
            const int cbase = (wm ? 640 : 0) + n0 + 64 * wn + 4 * g;
#pragma unroll
            for (int mt = 0; mt < 4; ++mt) {
                int t = t0 + 16 * mt + (lane & 15);
#pragma unroll
                for (int nt = 0; nt < 4; ++nt) {
                    u32x2 o;
                    o[0] = pack2(acc[mt][nt][0], acc[mt][nt][1]);
                    o[1] = pack2(acc[mt][nt][2], acc[mt][nt][3]);
                    *reinterpret_cast<u32x2*>(pq + (size_t)t * 1280 + cbase + 16 * nt) = o;
                }
            }
        }
        WAIT_V(0);
        ST(3);
    }
}

constexpr int K2_A = 8192, K2_B = 10240, K2_STAGE = K2_A + K2_B, K2_NST = 4;
constexpr int K2_LDS = K2_STAGE * K2_NST;
constexpr int CSTR = 2240;
constexpr int CW_BYTES = 4 * CSTR + 4640;
constexpr int OBUF_OFF = 4 * CW_BYTES;

#define ST3(ptr, i)
struct Frag2 { bf16x8 pf[2]; bf16x8 df[5]; };

__device__ __forceinline__ void gemmB_block(char* smem, int bid, const u16* __restrict__ pq,
    const u16* __restrict__ dr, const u16* __restrict__ di, u16* __restrict__ out, unsigned long long* dbg)
{
    ST3(dbg, 0);
    const int tid = threadIdx.x, lane = tid & 63;
    const int w = __builtin_amdgcn_readfirstlane(tid >> 6);
    const int xcd = bid & 7, idx = bid >> 3;
    const int tm = xcd * 4 + (idx >> 3), tn = idx & 7;
    const int t0 = tm * 64, n0 = tn * 80;
    const int g = lane >> 4;

    if (w >= 2) {
        const int l = w - 2;
        const u16* aptr[4];
#pragma unroll
        for (int i = 0; i < 4; ++i) {
            int row = 32 * l + 8 * i + (lane >> 3);
            int ch = (lane & 7) ^ ((row >> 1) & 7);
            aptr[i] = pq + (size_t)(t0 + row) * 1280 + ch * 8;
        }
        int boffs[5];
#pragma unroll
        for (int i = 0; i < 5; ++i) {
            int row = 40 * l + 8 * i + (lane >> 3);
            int ch = (lane & 7) ^ ((row >> 1) & 7);
            boffs[i] = (n0 + row) * D + ch * 8;
        }
        auto stage = [&](int kt, int buf) {
            char* sb = smem + buf * K2_STAGE;
            const u16* bsrc = (kt < 10) ? (dr + kt * 64) : (di + (kt - 10) * 64);
#pragma unroll
            for (int i = 0; i < 5; ++i) glds16(bsrc + boffs[i], sb + K2_A + (40 * l + 8 * i) * 128);
#pragma unroll
            for (int i = 0; i < 4; ++i) glds16(aptr[i] + kt * 64, sb + (32 * l + 8 * i) * 128);
        };
        stage(0, 0); stage(1, 1); stage(2, 2); stage(3, 3);
        WAIT_V(27); BAR();
        for (int kt4 = 0; kt4 < 16; kt4 += 4) {
            WAIT_V(18); BAR(); stage(kt4 + 4, 0);
            WAIT_V(18); BAR(); stage(kt4 + 5, 1);
            WAIT_V(18); BAR(); stage(kt4 + 6, 2);
            WAIT_V(18); BAR(); stage(kt4 + 7, 3);
        }
        WAIT_V(18); BAR();
        WAIT_V(9); BAR();
        WAIT_V(0); BAR();
        return;
    }

    f32x4 acc[2][5], y1[2][5];
#pragma unroll
    for (int a = 0; a < 2; ++a)
#pragma unroll
        for (int b = 0; b < 5; ++b) { acc[a][b] = (f32x4){0.f, 0.f, 0.f, 0.f}; y1[a][b] = acc[a][b]; }
    const unsigned lds0 = (unsigned)(uintptr_t)smem;
    unsigned aoff[2], boff[2];
#pragma unroll
    for (int ks = 0; ks < 2; ++ks) {
        int row = 32 * w + (lane & 15);
        aoff[ks] = lds0 + row * 128 + (((4 * ks + g) ^ ((row >> 1) & 7)) * 16);
        int r2 = (lane & 15);
        boff[ks] = lds0 + K2_A + r2 * 128 + (((4 * ks + g) ^ ((r2 >> 1) & 7)) * 16);
    }
    auto readfrag = [&](Frag2& f, int buf, int ks) {
        const unsigned a = aoff[ks] + buf * K2_STAGE, b = boff[ks] + buf * K2_STAGE;
        RD128(f.pf[0], a, 0); RD128(f.pf[1], a, 2048);
#pragma unroll
        for (int nt = 0; nt < 5; ++nt) RD128(f.df[nt], b, 2048 * nt);
    };
    auto pinfrag = [&](Frag2& f) {
        PIN(f.pf[0]); PIN(f.pf[1]);
#pragma unroll
        for (int nt = 0; nt < 5; ++nt) PIN(f.df[nt]);
    };
    auto mma = [&](Frag2& f) {
#pragma unroll
        for (int nt = 0; nt < 5; ++nt)
#pragma unroll
            for (int mt = 0; mt < 2; ++mt)
                acc[mt][nt] = __builtin_amdgcn_mfma_f32_16x16x32_bf16(f.df[nt], f.pf[mt], acc[mt][nt], 0, 0, 0);
    };
    auto mma_rd = [&](Frag2& fc, Frag2& fn, int buf, int ks) {
        const unsigned a = aoff[ks] + buf * K2_STAGE, b = boff[ks] + buf * K2_STAGE;
#pragma unroll
        for (int i = 0; i < 10; ++i) {
            const int nt = i >> 1, mt = i & 1;
            acc[mt][nt] = __builtin_amdgcn_mfma_f32_16x16x32_bf16(fc.df[nt], fc.pf[mt], acc[mt][nt], 0, 0, 0);
            SCHED();
            if (i < 2) { RD128(fn.pf[i], a, 2048 * i); }
            else if (i < 7) { RD128(fn.df[i - 2], b, 2048 * (i - 2)); }
            SCHED();
        }
    };
    auto sw = [&]() {
#pragma unroll
        for (int a = 0; a < 2; ++a)
#pragma unroll
            for (int nt = 0; nt < 5; ++nt) { y1[a][nt] = acc[a][nt]; acc[a][nt] = (f32x4){0.f, 0.f, 0.f, 0.f}; }
    };
    Frag2 f0, f1;
    BAR();
    ST3(dbg, 1);
    readfrag(f0, 0, 0);
#define KSTEP2(b, bn) do { WAIT_L0(); pinfrag(f0); mma_rd(f0, f1, b, 1); BAR(); pinfrag(f1); mma_rd(f1, f0, bn, 0); } while (0)
    for (int kt4 = 0; kt4 < 16; kt4 += 4) {
        KSTEP2(0, 1); KSTEP2(1, 2);
        if (kt4 == 8) sw();
        KSTEP2(2, 3); KSTEP2(3, 0);
    }
    KSTEP2(0, 1); KSTEP2(1, 2); KSTEP2(2, 3);
    WAIT_L0(); pinfrag(f0); mma_rd(f0, f1, 3, 1); WAIT_L0(); pinfrag(f1); mma(f1);
    ST3(dbg, 2);
#pragma unroll
    for (int mt = 0; mt < 2; ++mt) {
        const int t = t0 + 32 * w + 16 * mt + (lane & 15);
#pragma unroll
        for (int nt = 0; nt < 5; ++nt) {
            float o[4];
#pragma unroll
            for (int r = 0; r < 4; ++r) o[r] = rbf(y1[mt][nt][r]) - rbf(acc[mt][nt][r]);
            u32x2 ov; ov[0] = pack2(o[0], o[1]); ov[1] = pack2(o[2], o[3]);
            *reinterpret_cast<u32x2*>(out + (size_t)t * 1280 + 640 + n0 + 16 * nt + 4 * g) = ov;
        }
    }
    ST3(dbg, 3);
}

struct FragC { bf16x4 ga[4], gb[4]; bf16x8 F[8]; };

template<int ph>
__device__ __forceinline__ void conv_phase(f32x4 (&acc)[8], const unsigned gbase, const unsigned fbase)
{
        constexpr int NF = (ph == 0) ? 8 : 4;
        auto rd = [&](FragC& f, int q) {
            const unsigned ga = gbase + 64 * 8 - 64 * q;
            const unsigned fa_ = fbase + 64 * 8 - 64 * q;
#pragma unroll
            for (int pp = 0; pp < 4; ++pp) { RD64(f.ga[pp], ga, 64 * (24 - 8 * pp)); RD64(f.gb[pp], ga, 64 * (24 - 8 * pp) + 8); }
#pragma unroll
            for (int mm = 0; mm < NF; ++mm) RD128(f.F[mm], fa_, 512 * mm);
        };
        auto pin = [&](FragC& f) {
#pragma unroll
            for (int pp = 0; pp < 4; ++pp) { PIN(f.ga[pp]); PIN(f.gb[pp]); }
#pragma unroll
            for (int mm = 0; mm < NF; ++mm) PIN(f.F[mm]);
        };
        auto mm_rd = [&](FragC& fc, FragC& fn, int q, bool dord) {
            const unsigned ga = gbase + 64 * 8 - 64 * q;
            const unsigned fa_ = fbase + 64 * 8 - 64 * q;
            int ri = 0;
#pragma unroll
            for (int pp = 0; pp < 4; ++pp) {
                const int p = 4 * ph + pp;
                bf16x8 G = __builtin_shufflevector(fc.ga[pp], fc.gb[pp], 0, 1, 2, 3, 4, 5, 6, 7);
#pragma unroll
                for (int n = p; n < 8; ++n) {
                    acc[n] = __builtin_amdgcn_mfma_f32_16x16x32_bf16(G, fc.F[n - p], acc[n], 0, 0, 0);
                    SCHED();
                    if (dord) {
#pragma unroll
                        for (int rep = 0; rep < (ph == 0 ? 1 : 2); ++rep) {
                            if (ri < 8) { if (ri & 1) RD64(fn.gb[ri >> 1], ga, 64 * (24 - 8 * (ri >> 1)) + 8); else RD64(fn.ga[ri >> 1], ga, 64 * (24 - 8 * (ri >> 1))); }
                            else if (ri < 8 + NF) { RD128(fn.F[ri - 8], fa_, 512 * (ri - 8)); }
                            ++ri;
                        }
                    }
                    SCHED();
                }
            }
        };
        FragC fa, fb;
        bf16x8 F8; bf16x4 g64a, g64b;
        rd(fa, 0);
        RD128(F8, fbase, 0);
        RD64(g64a, gbase, 0); RD64(g64b, gbase, 8);
        WAIT_L0(); pin(fa); PIN(F8); PIN(g64a); PIN(g64b);
        mm_rd(fa, fb, 1, true);
        {
#pragma unroll
            for (int pp = 0; pp < 4; ++pp) {
                const int p = 4 * ph + pp;
                bf16x8 G = __builtin_shufflevector(fa.ga[pp], fa.gb[pp], 0, 1, 2, 3, 4, 5, 6, 7);
                if (p >= 1) acc[p - 1] = __builtin_amdgcn_mfma_f32_16x16x32_bf16(G, F8, acc[p - 1], 0, 0, 0);
            }
            if (ph == 1) {
                bf16x8 G64 = __builtin_shufflevector(g64a, g64b, 0, 1, 2, 3, 4, 5, 6, 7);
                acc[7] = __builtin_amdgcn_mfma_f32_16x16x32_bf16(G64, F8, acc[7], 0, 0, 0);
            }
        }
        SCHED();
        for (int q = 1; q < 7; q += 2) {
            WAIT_L0(); pin(fb); mm_rd(fb, fa, q + 1, true);
            WAIT_L0(); pin(fa); mm_rd(fa, fb, q + 2, true);
        }
        WAIT_L0(); pin(fb); mm_rd(fb, fa, 0, false);
}

__device__ __forceinline__ void conv_block(char* smem, int cb, const u16* __restrict__ ssm_k,
    const u16* __restrict__ ssm_D, const u16* __restrict__ kvT, const u16* __restrict__ qT, u16* __restrict__ out, unsigned long long* dbg)
{
    ST3(dbg, 0);
    const int tid = threadIdx.x, lane = tid & 63;
    const int w = __builtin_amdgcn_readfirstlane(tid >> 6);
    const int grp = (cb & 7) * 20 + (cb >> 3);
    const int d0 = grp * 4, d = d0 + w;
    char* cw = smem + w * CW_BYTES;
    u16* kvs = reinterpret_cast<u16*>(cw + 4 * CSTR);
    const u16* hrow = ssm_k + (size_t)d * S;
    const int g = lane >> 4, li = lane & 15;

    u32x4 hlo[2][3]; u32x2 hhi[2][3];
#pragma unroll
    for (int ph = 0; ph < 2; ++ph)
#pragma unroll
        for (int it = 0; it < 3; ++it) {
            int c = lane + 64 * it;
            int E = 1024 * ph + 1040 - 8 * c;
            hlo[ph][it] = (u32x4){0u, 0u, 0u, 0u};
            hhi[ph][it] = (u32x2){0u, 0u};
            if (c < 134 && E - 8 >= 0 && E - 8 < S) hlo[ph][it] = *reinterpret_cast<const u32x4*>(hrow + E - 8);
            if (c < 134 && E >= 0 && E < S) hhi[ph][it] = *reinterpret_cast<const u32x2*>(hrow + E);
        }
    u32x4 kvr[4];
#pragma unroll
    for (int i = 0; i < 4; ++i) kvr[i] = *reinterpret_cast<const u32x4*>(kvT + (size_t)d * S + 8 * (lane + 64 * i));
    u32x2 q4[8], kq4[8];
#pragma unroll
    for (int n = 0; n < 8; ++n) q4[n] = *reinterpret_cast<const u32x2*>(qT + (size_t)d * S + 256 * n + 16 * li + 4 * g);
#pragma unroll
    for (int n = 0; n < 8; ++n) kq4[n] = *reinterpret_cast<const u32x2*>(kvT + (size_t)d * S + 256 * n + 16 * li + 4 * g);
    const float Dd = bf2f(ssm_D[d]);

    f32x4 acc[8];
#pragma unroll
    for (int n = 0; n < 8; ++n) acc[n] = (f32x4){0.f, 0.f, 0.f, 0.f};

    const unsigned cw0 = (unsigned)(uintptr_t)cw;
    const unsigned gbase = cw0 + (li & 3) * CSTR + 2 * (1040 - 4 * (li >> 2) + 8 * g) - 64 * 32;
    const unsigned fbase = cw0 + 4 * CSTR + 2 * (256 + 16 * li + 8 * g) - 64 * 8;

    auto build = [&](int ph) {
#pragma unroll
        for (int it = 0; it < 3; ++it) {
            int c = lane + 64 * it;
            if (c < 134) {
                unsigned W[6] = {hlo[ph][it][0], hlo[ph][it][1], hlo[ph][it][2], hlo[ph][it][3], hhi[ph][it][0], hhi[ph][it][1]};
#pragma unroll
                for (int r = 0; r < 4; ++r) {
                    u32x4 o;
#pragma unroll
                    for (int m = 0; m < 4; ++m) {
                        if ((r & 1) == 0) {
                            int i = 4 + r / 2 - m;
                            o[m] = __builtin_amdgcn_perm(W[i - 1], W[i], 0x07060100u);
                        } else {
                            int i = (7 + r) / 2 - m;
                            o[m] = __builtin_amdgcn_perm(W[i], W[i], 0x01000302u);
                        }
                    }
                    *reinterpret_cast<u32x4*>(cw + r * CSTR + 16 * c) = o;
                }
            }
        }
    };
    build(0);
    {
        const u32x4 z = (u32x4){0u, 0u, 0u, 0u};
        if (lane < 32) *reinterpret_cast<u32x4*>(kvs + 8 * lane) = z;
        if (lane < 2) *reinterpret_cast<u32x4*>(kvs + 256 + 2048 + 8 * lane) = z;
#pragma unroll
        for (int i = 0; i < 4; ++i) *reinterpret_cast<u32x4*>(kvs + 256 + 8 * (lane + 64 * i)) = kvr[i];
    }

    WAIT_L0();
    __builtin_amdgcn_wave_barrier();
    ST3(dbg, 1);
    conv_phase<0>(acc, gbase, fbase);
    ST3(dbg, 2);
    WAIT_L0();
    __builtin_amdgcn_wave_barrier();
    build(1);
    WAIT_L0();
    __builtin_amdgcn_wave_barrier();
    ST3(dbg, 3);
    conv_phase<1>(acc, gbase, fbase);
    ST3(dbg, 4);

    char* obuf = smem + OBUF_OFF;
#pragma unroll
    for (int n = 0; n < 8; ++n) {
        int t = 256 * n + 16 * li + 4 * g;
        float o[4];
#pragma unroll
        for (int r = 0; r < 4; ++r) {
            unsigned kw = kq4[n][r >> 1], qw = q4[n][r >> 1];
            float kvf = (r & 1) ? hi16(kw) : lo16(kw);
            float qf = (r & 1) ? hi16(qw) : lo16(qw);
            float y = rbf(rbf(acc[n][r]) + rbf(kvf * Dd));
            o[r] = qf * y;
        }
        u32x2 ov; ov[0] = pack2(o[0], o[1]); ov[1] = pack2(o[2], o[3]);
        *reinterpret_cast<u32x2*>(obuf + w * 4112 + 2 * t) = ov;
    }
    __syncthreads();
    ST3(dbg, 5);
#pragma unroll
    for (int it = 0; it < 2; ++it) {
        int t = 4 * (tid + 256 * it);
        u32x2 c0 = *reinterpret_cast<const u32x2*>(obuf + 0 * 4112 + 2 * t);
        u32x2 c1 = *reinterpret_cast<const u32x2*>(obuf + 1 * 4112 + 2 * t);
        u32x2 c2 = *reinterpret_cast<const u32x2*>(obuf + 2 * 4112 + 2 * t);
        u32x2 c3 = *reinterpret_cast<const u32x2*>(obuf + 3 * 4112 + 2 * t);
#pragma unroll
        for (int r = 0; r < 4; ++r) {
            const unsigned sel = (r & 1) ? 0x07060302u : 0x05040100u;
            u32x2 o;
            o[0] = __builtin_amdgcn_perm(c1[r >> 1], c0[r >> 1], sel);
            o[1] = __builtin_amdgcn_perm(c3[r >> 1], c2[r >> 1], sel);
            *reinterpret_cast<u32x2*>(out + (size_t)(t + r) * 1280 + d0) = o;
        }
    }
    ST3(dbg, 6);
}

__global__ __launch_bounds__(256, 2) void k2_gemmB_conv(
    const u16* __restrict__ pq, const u16* __restrict__ dr, const u16* __restrict__ di,
    const u16* __restrict__ ssm_k, const u16* __restrict__ ssm_D,
    const u16* __restrict__ kvT, const u16* __restrict__ qT, u16* __restrict__ out)
{
    extern __shared__ __attribute__((aligned(16))) char smem[];
    const int bid = blockIdx.x;
    unsigned long long* dbg3 = (unsigned long long*)(const_cast<u16*>(qT) + (size_t)D * S) + 8192 + bid * 8;
    if (bid < 256) gemmB_block(smem, bid, pq, dr, di, out, dbg3);
    if (bid >= 256) conv_block(smem, bid - 256, ssm_k, ssm_D, kvT, qT, out, dbg3);
}

extern "C" void kernel_launch(void* const* d_in, const int* in_sizes, int n_in,
                              void* d_out, int out_size, void* d_ws, size_t ws_size,
                              hipStream_t stream) {
    const u16* q = (const u16*)d_in[0];
    const u16* k = (const u16*)d_in[1];
    const u16* v = (const u16*)d_in[2];
    const u16* shift_k = (const u16*)d_in[3];
    const u16* ssm_k = (const u16*)d_in[4];
    const u16* ssm_D = (const u16*)d_in[5];
    const u16* x = (const u16*)d_in[6];
    const u16* cr = (const u16*)d_in[7];
    const u16* ci = (const u16*)d_in[8];
    const u16* dr = (const u16*)d_in[9];
    const u16* di = (const u16*)d_in[10];
    u16* out = (u16*)d_out;
    u16* pq = (u16*)d_ws;
    u16* kvT = pq + (size_t)S * 1280;
    u16* qT = kvT + (size_t)D * S;
    (void)hipFuncSetAttribute((const void*)k1_gemmA_prep, hipFuncAttributeMaxDynamicSharedMemorySize, K1_LDS);
    (void)hipFuncSetAttribute((const void*)k2_gemmB_conv, hipFuncAttributeMaxDynamicSharedMemorySize, K2_LDS);
    k1_gemmA_prep<<<256, 512, K1_LDS, stream>>>(cr, ci, x, k, v, q, shift_k, pq, kvT, qT);
    k2_gemmB_conv<<<416, 256, K2_LDS, stream>>>(pq, dr, di, ssm_k, ssm_D, kvT, qT, out);
}
```

```cpp
#include <hip/hip_runtime.h>
#include <hip/hip_bf16.h>
#include <stdint.h>

typedef unsigned short u16;
typedef __attribute__((ext_vector_type(8))) short bf16x8;
typedef __attribute__((ext_vector_type(4))) short bf16x4;
typedef __attribute__((ext_vector_type(4))) float f32x4;
typedef __attribute__((ext_vector_type(4))) unsigned u32x4;
typedef __attribute__((ext_vector_type(2))) unsigned u32x2;

constexpr int S = 2048, D = 640;

#define WAIT_V(n) asm volatile("s_waitcnt vmcnt(" #n ")" ::: "memory")
#define WAIT_L0() asm volatile("s_waitcnt lgkmcnt(0)" ::: "memory")
#define BAR() do { asm volatile("s_waitcnt lgkmcnt(0)" ::: "memory"); __builtin_amdgcn_s_barrier(); asm volatile("" ::: "memory"); } while (0)
#define RD128(dst, addr, off) asm volatile("ds_read_b128 %0, %1 offset:%2" : "=v"(dst) : "v"(addr), "i"(off))
#define RD64(dst, addr, off) asm volatile("ds_read_b64 %0, %1 offset:%2" : "=v"(dst) : "v"(addr), "i"(off))
#define RDTR(dst, addr, off) asm volatile("ds_read_b64_tr_b16 %0, %1 offset:%2" : "=v"(dst) : "v"(addr), "i"(off))
#define PIN(x) asm volatile("" : "+v"(x))
#define SCHED() __builtin_amdgcn_sched_barrier(0)

#define ST(ptr, i)

__device__ __forceinline__ void glds16(const void* g, void* l) {
    __builtin_amdgcn_global_load_lds((const unsigned*)g, (unsigned*)l, 16, 0, 0);
}
__device__ __forceinline__ void glds4(const void* g, void* l) {
    __builtin_amdgcn_global_load_lds((const unsigned*)g, (unsigned*)l, 4, 0, 0);
}
__device__ __forceinline__ float bf2f(u16 x) { return __uint_as_float(((unsigned)x) << 16); }
__device__ __forceinline__ u16 f2bf(float x) { __hip_bfloat16 b = __float2bfloat16(x); return *reinterpret_cast<u16*>(&b); }
__device__ __forceinline__ float rbf(float x) { return bf2f(f2bf(x)); }
__device__ __forceinline__ unsigned pack2(float a, float b) { return (unsigned)f2bf(a) | ((unsigned)f2bf(b) << 16); }
__device__ __forceinline__ float lo16(unsigned w) { return __uint_as_float(w << 16); }
__device__ __forceinline__ float hi16(unsigned w) { return __uint_as_float(w & 0xffff0000u); }

constexpr int K1_A = 16384, K1_B = 16384, K1_STAGE = K1_A + K1_B, K1_NST = 4;
constexpr int K1_LDS = K1_STAGE * K1_NST + 1024;

struct Frag1 { bf16x8 cf[4]; bf16x4 xl[4], xh[4]; };

__global__ __launch_bounds__(512, 2) void k1_gemmA_prep(
    const u16* __restrict__ cr, const u16* __restrict__ ci, const u16* __restrict__ x,
    const u16* __restrict__ kk, const u16* __restrict__ vv, const u16* __restrict__ qq,
    const u16* __restrict__ shift_k, const u16* __restrict__ ssm_k, const u16* __restrict__ dr, const u16* __restrict__ di,
    u16* __restrict__ pq, u16* __restrict__ kvT, u16* __restrict__ qT)
{
    extern __shared__ __attribute__((aligned(16))) char smem[];
    const int tid = threadIdx.x, lane = tid & 63;
    const int w8 = __builtin_amdgcn_readfirstlane(tid >> 6);
    const int w = w8 & 3;
    const int bid = blockIdx.x;
    const int xcd = bid & 7, idx = bid >> 3;
    unsigned long long* dbg = (unsigned long long*)(pq + (size_t)S * 1280) + bid * 8;
    ST(dbg, 0);
#define PF0 8
    auto touch = [&](int L) {
        int ks = L / 36, gl = 36 * idx + L % 36;
        const char* p;
        if (gl < 512) p = (const char*)((gl >> 8) ? ci : cr) + (size_t)(256 * xcd + (gl & 255)) * 4096 + ks * 128;
        else { int l2 = gl - 512; p = (const char*)x + (size_t)(64 * ks + l2 / 10) * 1280 + (l2 % 10) * 128; }
        __builtin_amdgcn_global_load_lds((const unsigned*)p, (unsigned*)(smem + 4 * K1_STAGE + w * 256), 4, 0, 0);
    };
    if (idx >= 20) {
        if (w8 < 4) {
#pragma unroll
            for (int i = 0; i < 5; ++i) { int L = (i * 4 + w) * 64 + lane; if (L < 1152) touch(L); }
        }
    } else if (w8 < 4) {
#pragma unroll
        for (int i = 0; i < (PF0 * 36 + 255) / 256; ++i) { int L = (i * 4 + w) * 64 + lane; if (L < PF0 * 36) touch(L); }
    }

    if (idx >= 20) {
        u16* kvs = reinterpret_cast<u16*>(smem);
        u16* qs = kvs + 64 * 72;
        for (int tile = xcd * 12 + (idx - 20); tile < 320; tile += 96) {
            const int tt0 = (tile / 10) * 64, d0 = (tile % 10) * 64;
            {
                int r = tid >> 3, c = tid & 7;
                int t = tt0 + r, d = d0 + 8 * c;
                u32x4 kr[4];
#pragma unroll
                for (int j = 0; j < 4; ++j) {
                    int ts = t + j - 3;
                    kr[j] = *reinterpret_cast<const u32x4*>(kk + (size_t)(ts < 0 ? 0 : ts) * D + d);
                    if (ts < 0) kr[j] = (u32x4){0u, 0u, 0u, 0u};
                }
                u32x4 vr = *reinterpret_cast<const u32x4*>(vv + (size_t)t * D + d);
                u32x4 qr = *reinterpret_cast<const u32x4*>(qq + (size_t)t * D + d);
                u32x4 sk[4];
#pragma unroll
                for (int j = 0; j < 4; ++j) sk[j] = *reinterpret_cast<const u32x4*>(shift_k + d * 4 + 8 * j);
#pragma unroll
                for (int e = 0; e < 8; ++e) {
                    float a = 0.f;
#pragma unroll
                    for (int j = 0; j < 4; ++j) {
                        unsigned kw = kr[j][e >> 1];
                        float kf = (e & 1) ? hi16(kw) : lo16(kw);
                        int si = e * 4 + 3 - j;
                        unsigned sw = sk[si >> 3][(si >> 1) & 3];
                        float sf = (si & 1) ? hi16(sw) : lo16(sw);
                        a = rbf(a + rbf(kf * sf));
                    }
                    unsigned vw = vr[e >> 1];
                    float vf = (e & 1) ? hi16(vw) : lo16(vw);
                    kvs[(8 * c + e) * 72 + r] = f2bf(a * vf);
                    unsigned qw = qr[e >> 1];
                    qs[(8 * c + e) * 72 + r] = (u16)((e & 1) ? (qw >> 16) : (qw & 0xffff));
                }
            }
            __syncthreads();
            {
                int dl = tid >> 3, c = tid & 7;
                u32x4 a = *reinterpret_cast<const u32x4*>(kvs + dl * 72 + 8 * c);
                u32x4 b = *reinterpret_cast<const u32x4*>(qs + dl * 72 + 8 * c);
                *reinterpret_cast<u32x4*>(kvT + (size_t)(d0 + dl) * S + tt0 + 8 * c) = a;
                *reinterpret_cast<u32x4*>(qT + (size_t)(d0 + dl) * S + tt0 + 8 * c) = b;
            }
            __syncthreads();
        }
        for (int L = (idx - 20) * 512 + tid; L < 15360; L += 12 * 512) {
            const char* p = (L < 6400) ? (const char*)dr + (size_t)L * 128 : (L < 12800) ? (const char*)di + (size_t)(L - 6400) * 128
                          : (const char*)ssm_k + (size_t)xcd * 80 * 4096 + (size_t)(L - 12800) * 128;
            glds4(p, smem + 4 * K1_STAGE + w * 256);
        }
        WAIT_V(0);
        return;
    }

    const int tm = xcd * 4 + idx / 5, tn = idx % 5;
    const int t0 = tm * 64, n0 = tn * 128;

    if (w8 >= 4) {
        const u16* aptr[4];
#pragma unroll
        for (int i = 0; i < 4; ++i) {
            int row = 32 * w + 8 * i + (lane >> 3);
            int ch = (lane & 7) ^ ((row >> 1) & 7);
            aptr[i] = (row < 64 ? cr : ci) + (size_t)(t0 + (row & 63)) * S + ch * 8;
        }
        const u16* bptr[4];
#pragma unroll
        for (int i = 0; i < 4; ++i) {
            int line = (w * 4 + i) * 8 + (lane >> 3);
            int kb = line >> 3, c64 = (line >> 2) & 1, q = line & 3;
            int c = lane & 7;
            int f = ((q >> 1) & 1) | (((kb >> 1) & 1) << 1);
            int cg = ((((c >> 1) ^ f) << 1) | (c & 1));
            bptr[i] = x + (4 * kb + q) * D + n0 + 64 * c64 + 8 * cg;
        }
#define STAG 0
        const int stag = STAG;
        auto stage = [&](int kt_, int buf) {
            const int kt = (kt_ + stag) & 31;
            char* sb = smem + buf * K1_STAGE;
#pragma unroll
            for (int i = 0; i < 4; ++i) glds16(aptr[i] + kt * 64, sb + (32 * w + 8 * i) * 128);
#pragma unroll
            for (int i = 0; i < 4; ++i) glds16(bptr[i] + kt * 64 * D, sb + K1_A + (w * 4 + i) * 1024);
        };
        stage(0, 0); stage(1, 1); stage(2, 2); stage(3, 3);
        WAIT_V(24); BAR();
        for (int kt4 = 0; kt4 < 28; kt4 += 4) {
            WAIT_V(16); BAR(); stage(kt4 + 4, 0);
            WAIT_V(16); BAR(); stage(kt4 + 5, 1);
            WAIT_V(16); BAR(); stage(kt4 + 6, 2);
            WAIT_V(16); BAR(); stage(kt4 + 7, 3);
        }
        WAIT_V(16); BAR();
        WAIT_V(8); BAR();
        WAIT_V(0); BAR();
    } else {
        const int wm = w & 1, wn = w >> 1;
        f32x4 acc[4][4];
#pragma unroll
        for (int a = 0; a < 4; ++a)
#pragma unroll
            for (int b = 0; b < 4; ++b) acc[a][b] = (f32x4){0.f, 0.f, 0.f, 0.f};
        const int g = lane >> 4;
        const unsigned lds0 = (unsigned)(uintptr_t)smem;
        unsigned aoff[2];
#pragma unroll
        for (int ks = 0; ks < 2; ++ks) {
            int row = 64 * wm + (lane & 15);
            int ch = (4 * ks + g) ^ ((row >> 1) & 7);
            aoff[ks] = lds0 + row * 128 + ch * 16;
        }
        unsigned boff[4];
        {
            int q = (lane & 15) >> 2, p = lane & 3;
            int f = ((q >> 1) & 1) | ((g & 1) << 1);
#pragma unroll
            for (int nt = 0; nt < 4; ++nt)
                boff[nt] = lds0 + K1_A + (2 * g) * 1024 + wn * 512 + q * 128 + ((nt ^ f) * 32) + p * 8;
        }
        auto readfrag = [&](Frag1& f, int buf, int ks) {
            const unsigned bo = buf * K1_STAGE;
            const unsigned a0 = aoff[ks] + bo;
            RD128(f.cf[0], a0, 0); RD128(f.cf[1], a0, 2048); RD128(f.cf[2], a0, 4096); RD128(f.cf[3], a0, 6144);
#pragma unroll
            for (int nt = 0; nt < 4; ++nt) {
                const unsigned b0 = boff[nt] + bo;
                if (ks == 0) { RDTR(f.xl[nt], b0, 0); RDTR(f.xh[nt], b0, 1024); }
                else         { RDTR(f.xl[nt], b0, 8192); RDTR(f.xh[nt], b0, 9216); }
            }
        };
        auto pinfrag = [&](Frag1& f) {
#pragma unroll
            for (int i = 0; i < 4; ++i) { PIN(f.cf[i]); PIN(f.xl[i]); PIN(f.xh[i]); }
        };
        auto mma = [&](Frag1& f) {
#pragma unroll
            for (int nt = 0; nt < 4; ++nt) {
                bf16x8 xf = __builtin_shufflevector(f.xl[nt], f.xh[nt], 0, 1, 2, 3, 4, 5, 6, 7);
#pragma unroll
                for (int mt = 0; mt < 4; ++mt)
                    acc[mt][nt] = __builtin_amdgcn_mfma_f32_16x16x32_bf16(xf, f.cf[mt], acc[mt][nt], 0, 0, 0);
            }
        };
        auto mma_rd = [&](Frag1& fc, Frag1& fn, int buf, int ks) {
            const unsigned bo = buf * K1_STAGE;
            const unsigned a0 = aoff[ks] + bo;
#pragma unroll
            for (int i = 0; i < 16; ++i) {
                const int nt = i >> 2, mt = i & 3;
                bf16x8 xf = __builtin_shufflevector(fc.xl[nt], fc.xh[nt], 0, 1, 2, 3, 4, 5, 6, 7);
                acc[mt][nt] = __builtin_amdgcn_mfma_f32_16x16x32_bf16(xf, fc.cf[mt], acc[mt][nt], 0, 0, 0);
                SCHED();
                if (i < 4) { RD128(fn.cf[i], a0, 2048 * i); }
                else if (i < 12) {
                    const int j = (i - 4) >> 1;
                    const unsigned b0 = boff[j] + bo;
                    if ((i & 1) == 0) { if (ks == 0) RDTR(fn.xl[j], b0, 0); else RDTR(fn.xl[j], b0, 8192); }
                    else { if (ks == 0) RDTR(fn.xh[j], b0, 1024); else RDTR(fn.xh[j], b0, 9216); }
                }
                SCHED();
            }
        };
        Frag1 f0, f1;
        BAR();
        ST(dbg, 1);
        readfrag(f0, 0, 0);
#define KSTEP(b, bn) do { WAIT_L0(); pinfrag(f0); mma_rd(f0, f1, b, 1); BAR(); pinfrag(f1); mma_rd(f1, f0, bn, 0); } while (0)
        for (int kt4 = 0; kt4 < 28; kt4 += 4) {
            { int L = (kt4 + PF0 + w) * 36 + lane; if (lane < 36 && L < 1152) touch(L); }
            KSTEP(0, 1); KSTEP(1, 2); KSTEP(2, 3); KSTEP(3, 0);
        }
        KSTEP(0, 1); KSTEP(1, 2); KSTEP(2, 3);
        WAIT_L0(); pinfrag(f0); mma_rd(f0, f1, 3, 1); WAIT_L0(); pinfrag(f1); mma(f1);
        ST(dbg, 2);
        {
            const int cbase = (wm ? 640 : 0) + n0 + 64 * wn + 4 * g;
#pragma unroll
            for (int mt = 0; mt < 4; ++mt) {
                int t = t0 + 16 * mt + (lane & 15);
#pragma unroll
                for (int nt = 0; nt < 4; ++nt) {
                    u32x2 o;
                    o[0] = pack2(acc[mt][nt][0], acc[mt][nt][1]);
                    o[1] = pack2(acc[mt][nt][2], acc[mt][nt][3]);
                    *reinterpret_cast<u32x2*>(pq + (size_t)t * 1280 + cbase + 16 * nt) = o;
                }
            }
        }
        WAIT_V(0);
        ST(dbg, 3);
    }
}

constexpr int KVROW = 4640;
constexpr int CSTR = 2240;
constexpr int CV_TOEP = 8 * KVROW;
constexpr int CV_OBUF = CV_TOEP + 8 * 4 * CSTR;
constexpr int OBROW = 2064;
constexpr int K2_A = 8192, K2_B = 16384, K2_STAGE = K2_A + K2_B, K2_NST = 4;
constexpr int K2_LDS = CV_OBUF + 8 * OBROW;

struct HRegs { u32x4 lo[2][3]; u32x2 hi[2][3]; };

__device__ __forceinline__ void load_h(HRegs& h, const u16* __restrict__ hrow, int lane)
{
#pragma unroll
    for (int ph = 0; ph < 2; ++ph)
#pragma unroll
        for (int it = 0; it < 3; ++it) {
            int c = lane + 64 * it;
            int E = 1024 * ph + 1040 - 8 * c;
            h.lo[ph][it] = (u32x4){0u, 0u, 0u, 0u};
            h.hi[ph][it] = (u32x2){0u, 0u};
            if (c < 134 && E - 8 >= 0 && E - 8 < S) h.lo[ph][it] = *reinterpret_cast<const u32x4*>(hrow + E - 8);
            if (c < 134 && E >= 0 && E < S) h.hi[ph][it] = *reinterpret_cast<const u32x2*>(hrow + E);
        }
}

__device__ __forceinline__ void build_toep(const HRegs& h, int ph, char* cw, int lane)
{
#pragma unroll
    for (int it = 0; it < 3; ++it) {
        int c = lane + 64 * it;
        if (c < 134) {
            unsigned W[6] = {h.lo[ph][it][0], h.lo[ph][it][1], h.lo[ph][it][2], h.lo[ph][it][3], h.hi[ph][it][0], h.hi[ph][it][1]};
#pragma unroll
            for (int r = 0; r < 4; ++r) {
                u32x4 o;
#pragma unroll
                for (int m = 0; m < 4; ++m) {
                    if ((r & 1) == 0) { int i = 4 + r / 2 - m; o[m] = __builtin_amdgcn_perm(W[i - 1], W[i], 0x07060100u); }
                    else { int i = (7 + r) / 2 - m; o[m] = __builtin_amdgcn_perm(W[i], W[i], 0x01000302u); }
                }
                *reinterpret_cast<u32x4*>(cw + r * CSTR + 16 * c) = o;
            }
        }
    }
}

struct FragC { bf16x4 ga[4], gb[4]; bf16x8 F[8]; };

template<int ph>
__device__ __forceinline__ void conv_phase(f32x4 (&acc)[8], const unsigned gbase, const unsigned fbase)
{
        constexpr int NF = (ph == 0) ? 8 : 4;
        auto rd = [&](FragC& f, int q) {
            const unsigned ga = gbase + 64 * 8 - 64 * q;
            const unsigned fa_ = fbase + 64 * 8 - 64 * q;
#pragma unroll
            for (int pp = 0; pp < 4; ++pp) { RD64(f.ga[pp], ga, 64 * (24 - 8 * pp)); RD64(f.gb[pp], ga, 64 * (24 - 8 * pp) + 8); }
#pragma unroll
            for (int mm = 0; mm < NF; ++mm) RD128(f.F[mm], fa_, 512 * mm);
        };
        auto pin = [&](FragC& f) {
#pragma unroll
            for (int pp = 0; pp < 4; ++pp) { PIN(f.ga[pp]); PIN(f.gb[pp]); }
#pragma unroll
            for (int mm = 0; mm < NF; ++mm) PIN(f.F[mm]);
        };
        auto mm_rd = [&](FragC& fc, FragC& fn, int q, bool dord) {
            const unsigned ga = gbase + 64 * 8 - 64 * q;
            const unsigned fa_ = fbase + 64 * 8 - 64 * q;
            int ri = 0;
#pragma unroll
            for (int pp = 0; pp < 4; ++pp) {
                const int p = 4 * ph + pp;
                bf16x8 G = __builtin_shufflevector(fc.ga[pp], fc.gb[pp], 0, 1, 2, 3, 4, 5, 6, 7);
#pragma unroll
                for (int n = p; n < 8; ++n) {
                    acc[n] = __builtin_amdgcn_mfma_f32_16x16x32_bf16(G, fc.F[n - p], acc[n], 0, 0, 0);
                    SCHED();
                    if (dord) {
#pragma unroll
                        for (int rep = 0; rep < (ph == 0 ? 1 : 2); ++rep) {
                            if (ri < 8) { if (ri & 1) RD64(fn.gb[ri >> 1], ga, 64 * (24 - 8 * (ri >> 1)) + 8); else RD64(fn.ga[ri >> 1], ga, 64 * (24 - 8 * (ri >> 1))); }
                            else if (ri < 8 + NF) { RD128(fn.F[ri - 8], fa_, 512 * (ri - 8)); }
                            ++ri;
                        }
                    }
                    SCHED();
                }
            }
        };
        FragC fa, fb;
        bf16x8 F8; bf16x4 g64a, g64b;
        rd(fa, 0);
        RD128(F8, fbase, 0);
        RD64(g64a, gbase, 0); RD64(g64b, gbase, 8);
        WAIT_L0(); pin(fa); PIN(F8); PIN(g64a); PIN(g64b);
        mm_rd(fa, fb, 1, true);
        {
#pragma unroll
            for (int pp = 0; pp < 4; ++pp) {
                const int p = 4 * ph + pp;
                bf16x8 G = __builtin_shufflevector(fa.ga[pp], fa.gb[pp], 0, 1, 2, 3, 4, 5, 6, 7);
                if (pp >= 1) acc[p - 1] = __builtin_amdgcn_mfma_f32_16x16x32_bf16(G, F8, acc[p - 1], 0, 0, 0);
            }
            {
                bf16x8 G64 = __builtin_shufflevector(g64a, g64b, 0, 1, 2, 3, 4, 5, 6, 7);
                acc[4 * ph + 3] = __builtin_amdgcn_mfma_f32_16x16x32_bf16(G64, F8, acc[4 * ph + 3], 0, 0, 0);
            }
        }
        SCHED();
        for (int q = 1; q < 7; q += 2) {
            WAIT_L0(); pin(fb); mm_rd(fb, fa, q + 1, true);
            WAIT_L0(); pin(fa); mm_rd(fa, fb, q + 2, true);
        }
        WAIT_L0(); pin(fb); mm_rd(fb, fa, 0, false);
}

__device__ __forceinline__ void conv_store(const char* buf, int rowstride, u16* __restrict__ out, int d0, int tl, int t)
{
    u32x2 y[8];
#pragma unroll
    for (int e = 0; e < 8; ++e) y[e] = *reinterpret_cast<const u32x2*>(buf + e * rowstride + 2 * tl);
#pragma unroll
    for (int r = 0; r < 4; ++r) {
        const unsigned sel = (r & 1) ? 0x07060302u : 0x05040100u;
        u32x4 o;
#pragma unroll
        for (int m = 0; m < 4; ++m) o[m] = __builtin_amdgcn_perm(y[2 * m + 1][r >> 1], y[2 * m][r >> 1], sel);
        *reinterpret_cast<u32x4*>(out + (size_t)(t + r) * 1280 + d0) = o;
    }
}

__device__ __forceinline__ void conv_block(char* smem, int grp, const u16* __restrict__ ssm_k,
    const u16* __restrict__ ssm_D, const u16* __restrict__ kvT, const u16* __restrict__ qT, u16* __restrict__ out, unsigned long long* dbg)
{
    const int tid = threadIdx.x, lane = tid & 63;
    const int w8 = __builtin_amdgcn_readfirstlane(tid >> 6);
    const int d0 = grp * 8, d = d0 + w8;
    char* cw = smem + CV_TOEP + w8 * (4 * CSTR);
    char* kvrow = smem + w8 * KVROW;
    char* obuf = smem + CV_OBUF;
    const int g = lane >> 4, li = lane & 15;

    HRegs h;
    load_h(h, ssm_k + (size_t)d * S, lane);
    u32x4 kvr[4];
#pragma unroll
    for (int i = 0; i < 4; ++i) kvr[i] = *reinterpret_cast<const u32x4*>(kvT + (size_t)d * S + 8 * (lane + 64 * i));
    u32x2 q4[8];
#pragma unroll
    for (int n = 0; n < 8; ++n) q4[n] = *reinterpret_cast<const u32x2*>(qT + (size_t)d * S + 256 * n + 16 * li + 4 * g);
    const float Dd = bf2f(ssm_D[d]);

    f32x4 acc[8];
#pragma unroll
    for (int n = 0; n < 8; ++n) acc[n] = (f32x4){0.f, 0.f, 0.f, 0.f};
    const unsigned gbase = (unsigned)(uintptr_t)cw + (li & 3) * CSTR + 2 * (1040 - 4 * (li >> 2) + 8 * g) - 64 * 32;
    const unsigned fbase = (unsigned)(uintptr_t)kvrow + 2 * (256 + 16 * li + 8 * g) - 64 * 8;

    auto epi = [&](int n0_) {
        u32x2 kv4[4];
#pragma unroll
        for (int n = 0; n < 4; ++n) kv4[n] = *reinterpret_cast<const u32x2*>(kvrow + 2 * (256 + 256 * (n0_ + n) + 16 * li + 4 * g));
#pragma unroll
        for (int n = 0; n < 4; ++n) {
            int t = 256 * (n0_ + n) + 16 * li + 4 * g;
            float o[4];
#pragma unroll
            for (int r = 0; r < 4; ++r) {
                unsigned kw = kv4[n][r >> 1], qw = q4[n0_ + n][r >> 1];
                float kvf = (r & 1) ? hi16(kw) : lo16(kw);
                float qf = (r & 1) ? hi16(qw) : lo16(qw);
                o[r] = qf * rbf(rbf(acc[n0_ + n][r]) + rbf(kvf * Dd));
            }
            u32x2 ov; ov[0] = pack2(o[0], o[1]); ov[1] = pack2(o[2], o[3]);
            if (n0_ == 0) *reinterpret_cast<u32x2*>(obuf + w8 * OBROW + 2 * t) = ov;
            else *reinterpret_cast<u32x2*>(kvrow + 2 * (256 + t)) = ov;
        }
    };
    build_toep(h, 0, cw, lane);
    {
        const u32x4 z = (u32x4){0u, 0u, 0u, 0u};
        if (lane < 32) *reinterpret_cast<u32x4*>(kvrow + 16 * lane) = z;
        if (lane < 2) *reinterpret_cast<u32x4*>(kvrow + 2 * (256 + 2048) + 16 * lane) = z;
#pragma unroll
        for (int i = 0; i < 4; ++i) *reinterpret_cast<u32x4*>(kvrow + 512 + 16 * (lane + 64 * i)) = kvr[i];
    }
    WAIT_L0();
    __builtin_amdgcn_wave_barrier();
    ST(dbg, 1);
    conv_phase<0>(acc, gbase, fbase);
    ST(dbg, 2);
    WAIT_L0();
    __builtin_amdgcn_wave_barrier();
    build_toep(h, 1, cw, lane);
    epi(0);
    __syncthreads();
    if (tid < 256) conv_store(obuf, OBROW, out, d0, 4 * tid, 4 * tid);
    ST(dbg, 3);
    conv_phase<1>(acc, gbase, fbase);
    WAIT_L0();
    __builtin_amdgcn_wave_barrier();
    epi(4);
    __syncthreads();
    if (tid >= 256) conv_store(smem + 512, KVROW, out, d0, 4 * tid, 4 * tid);
    ST(dbg, 4);
}

struct Frag2 { bf16x8 pf[2]; bf16x8 df[4]; };

__global__ __launch_bounds__(512, 2) void k2_gemmB_conv(
    const u16* __restrict__ pq, const u16* __restrict__ dr, const u16* __restrict__ di,
    const u16* __restrict__ ssm_k, const u16* __restrict__ ssm_D,
    const u16* __restrict__ kvT, const u16* __restrict__ qT, u16* __restrict__ out)
{
    extern __shared__ __attribute__((aligned(16))) char smem[];
    const int bid = blockIdx.x;
    const int tid = threadIdx.x, lane = tid & 63;
    const int w8 = __builtin_amdgcn_readfirstlane(tid >> 6);
    const int w = w8 & 3;
    const int xcd = bid & 7, idx = bid >> 3;
    unsigned long long* dbg = (unsigned long long*)(const_cast<u16*>(pq) + (size_t)S * 1280) + 4096 + bid * 8;
    ST(dbg, 0);
    if (idx < 10) {
        conv_block(smem, xcd * 10 + idx, ssm_k, ssm_D, kvT, qT, out, dbg);
        return;
    }
    const int gi = idx - 10;
    const int tm = xcd * 4 + gi / 5, tn = gi % 5;
    const int t0 = tm * 64, n0 = tn * 128;
    const int g = lane >> 4;

    if (w8 >= 4) {
        const u16* aptr[2];
#pragma unroll
        for (int i = 0; i < 2; ++i) {
            int row = 16 * w + 8 * i + (lane >> 3);
            int ch = (lane & 7) ^ ((row >> 1) & 7);
            aptr[i] = pq + (size_t)(t0 + row) * 1280 + ch * 8;
        }
        int boffs[4];
#pragma unroll
        for (int i = 0; i < 4; ++i) {
            int row = 32 * w + 8 * i + (lane >> 3);
            int ch = (lane & 7) ^ ((row >> 1) & 7);
            boffs[i] = (n0 + row) * D + ch * 8;
        }
        auto stage = [&](int kt, int buf) {
            char* sb = smem + buf * K2_STAGE;
            const u16* bsrc = (kt < 10) ? (dr + kt * 64) : (di + (kt - 10) * 64);
#pragma unroll
            for (int i = 0; i < 4; ++i) glds16(bsrc + boffs[i], sb + K2_A + (32 * w + 8 * i) * 128);
#pragma unroll
            for (int i = 0; i < 2; ++i) glds16(aptr[i] + kt * 64, sb + (16 * w + 8 * i) * 128);
        };
        stage(0, 0); stage(1, 1); stage(2, 2); stage(3, 3);
        WAIT_V(18); BAR();
        for (int kt4 = 0; kt4 < 16; kt4 += 4) {
            WAIT_V(12); BAR(); stage(kt4 + 4, 0);
            WAIT_V(12); BAR(); stage(kt4 + 5, 1);
            WAIT_V(12); BAR(); stage(kt4 + 6, 2);
            WAIT_V(12); BAR(); stage(kt4 + 7, 3);
        }
        WAIT_V(12); BAR();
        WAIT_V(6); BAR();
        WAIT_V(0); BAR();
        return;
    }

    const int wm = w & 1, wn = w >> 1;
    f32x4 acc[2][4], y1[2][4];
#pragma unroll
    for (int a = 0; a < 2; ++a)
#pragma unroll
        for (int b = 0; b < 4; ++b) { acc[a][b] = (f32x4){0.f, 0.f, 0.f, 0.f}; y1[a][b] = acc[a][b]; }
    const unsigned lds0 = (unsigned)(uintptr_t)smem;
    unsigned aoff[2], boff[2];
#pragma unroll
    for (int ks = 0; ks < 2; ++ks) {
        int row = 32 * wm + (lane & 15);
        aoff[ks] = lds0 + row * 128 + (((4 * ks + g) ^ ((row >> 1) & 7)) * 16);
        int r2 = 64 * wn + (lane & 15);
        boff[ks] = lds0 + K2_A + r2 * 128 + (((4 * ks + g) ^ ((r2 >> 1) & 7)) * 16);
    }
    auto readfrag = [&](Frag2& f, int buf, int ks) {
        const unsigned a = aoff[ks] + buf * K2_STAGE, b = boff[ks] + buf * K2_STAGE;
        RD128(f.pf[0], a, 0); RD128(f.pf[1], a, 2048);
#pragma unroll
        for (int nt = 0; nt < 4; ++nt) RD128(f.df[nt], b, 2048 * nt);
    };
    auto pinfrag = [&](Frag2& f) {
        PIN(f.pf[0]); PIN(f.pf[1]);
#pragma unroll
        for (int nt = 0; nt < 4; ++nt) PIN(f.df[nt]);
    };
    auto mma = [&](Frag2& f) {
#pragma unroll
        for (int nt = 0; nt < 4; ++nt)
#pragma unroll
            for (int mt = 0; mt < 2; ++mt)
                acc[mt][nt] = __builtin_amdgcn_mfma_f32_16x16x32_bf16(f.df[nt], f.pf[mt], acc[mt][nt], 0, 0, 0);
    };
    auto mma_rd = [&](Frag2& fc, Frag2& fn, int buf, int ks) {
        const unsigned a = aoff[ks] + buf * K2_STAGE, b = boff[ks] + buf * K2_STAGE;
#pragma unroll
        for (int i = 0; i < 8; ++i) {
            const int nt = i >> 1, mt = i & 1;
            acc[mt][nt] = __builtin_amdgcn_mfma_f32_16x16x32_bf16(fc.df[nt], fc.pf[mt], acc[mt][nt], 0, 0, 0);
            SCHED();
            if (i < 2) { RD128(fn.pf[i], a, 2048 * i); }
            else if (i < 6) { RD128(fn.df[i - 2], b, 2048 * (i - 2)); }
            SCHED();
        }
    };
    auto sw = [&]() {
#pragma unroll
        for (int a = 0; a < 2; ++a)
#pragma unroll
            for (int nt = 0; nt < 4; ++nt) { y1[a][nt] = acc[a][nt]; acc[a][nt] = (f32x4){0.f, 0.f, 0.f, 0.f}; }
    };
    Frag2 f0, f1;
    BAR();
    ST(dbg, 1);
    readfrag(f0, 0, 0);
#define KSTEP2(b, bn) do { WAIT_L0(); pinfrag(f0); mma_rd(f0, f1, b, 1); BAR(); pinfrag(f1); mma_rd(f1, f0, bn, 0); } while (0)
    for (int kt4 = 0; kt4 < 16; kt4 += 4) {
        KSTEP2(0, 1); KSTEP2(1, 2);
        if (kt4 == 8) sw();
        KSTEP2(2, 3); KSTEP2(3, 0);
    }
    KSTEP2(0, 1); KSTEP2(1, 2); KSTEP2(2, 3);
    WAIT_L0(); pinfrag(f0); mma_rd(f0, f1, 3, 1); WAIT_L0(); pinfrag(f1); mma(f1);
    ST(dbg, 2);
#pragma unroll
    for (int mt = 0; mt < 2; ++mt) {
        const int t = t0 + 32 * wm + 16 * mt + (lane & 15);
#pragma unroll
        for (int nt = 0; nt < 4; ++nt) {
            float o[4];
#pragma unroll
            for (int r = 0; r < 4; ++r) o[r] = rbf(y1[mt][nt][r]) - rbf(acc[mt][nt][r]);
            u32x2 ov; ov[0] = pack2(o[0], o[1]); ov[1] = pack2(o[2], o[3]);
            *reinterpret_cast<u32x2*>(out + (size_t)t * 1280 + 640 + n0 + 64 * wn + 16 * nt + 4 * g) = ov;
        }
    }
    ST(dbg, 3);
}

extern "C" void kernel_launch(void* const* d_in, const int* in_sizes, int n_in,
                              void* d_out, int out_size, void* d_ws, size_t ws_size,
                              hipStream_t stream) {
    const u16* q = (const u16*)d_in[0];
    const u16* k = (const u16*)d_in[1];
    const u16* v = (const u16*)d_in[2];
    const u16* shift_k = (const u16*)d_in[3];
    const u16* ssm_k = (const u16*)d_in[4];
    const u16* ssm_D = (const u16*)d_in[5];
    const u16* x = (const u16*)d_in[6];
    const u16* cr = (const u16*)d_in[7];
    const u16* ci = (const u16*)d_in[8];
    const u16* dr = (const u16*)d_in[9];
    const u16* di = (const u16*)d_in[10];
    u16* out = (u16*)d_out;
    u16* pq = (u16*)d_ws;
    u16* kvT = pq + (size_t)S * 1280 + 65536;
    u16* qT = kvT + (size_t)D * S;
    (void)hipFuncSetAttribute((const void*)k1_gemmA_prep, hipFuncAttributeMaxDynamicSharedMemorySize, K1_LDS);
    (void)hipFuncSetAttribute((const void*)k2_gemmB_conv, hipFuncAttributeMaxDynamicSharedMemorySize, K2_LDS);
    k1_gemmA_prep<<<256, 512, K1_LDS, stream>>>(cr, ci, x, k, v, q, shift_k, ssm_k, dr, di, pq, kvT, qT);
    k2_gemmB_conv<<<240, 512, K2_LDS, stream>>>(pq, dr, di, ssm_k, ssm_D, kvT, qT, out);
}
```

```cpp
#include <hip/hip_runtime.h>
#include <hip/hip_bf16.h>
#include <stdint.h>

typedef unsigned short u16;
typedef __attribute__((ext_vector_type(8))) short bf16x8;
typedef __attribute__((ext_vector_type(4))) short bf16x4;
typedef __attribute__((ext_vector_type(4))) float f32x4;
typedef __attribute__((ext_vector_type(4))) unsigned u32x4;
typedef __attribute__((ext_vector_type(2))) unsigned u32x2;

constexpr int S = 2048, D = 640;

#define WAIT_V(n) asm volatile("s_waitcnt vmcnt(" #n ")" ::: "memory")
#define WAIT_L0() asm volatile("s_waitcnt lgkmcnt(0)" ::: "memory")
#define BAR() do { asm volatile("s_waitcnt lgkmcnt(0)" ::: "memory"); __builtin_amdgcn_s_barrier(); asm volatile("" ::: "memory"); } while (0)
#define RD128(dst, addr, off) asm volatile("ds_read_b128 %0, %1 offset:%2" : "=v"(dst) : "v"(addr), "i"(off))
#define RD64(dst, addr, off) asm volatile("ds_read_b64 %0, %1 offset:%2" : "=v"(dst) : "v"(addr), "i"(off))
#define RDTR(dst, addr, off) asm volatile("ds_read_b64_tr_b16 %0, %1 offset:%2" : "=v"(dst) : "v"(addr), "i"(off))
#define PIN(x) asm volatile("" : "+v"(x))
#define SCHED() __builtin_amdgcn_sched_barrier(0)


__device__ __forceinline__ void glds16(const void* g, void* l) {
    __builtin_amdgcn_global_load_lds((const unsigned*)g, (unsigned*)l, 16, 0, 0);
}
__device__ __forceinline__ void glds4(const void* g, void* l) {
    __builtin_amdgcn_global_load_lds((const unsigned*)g, (unsigned*)l, 4, 0, 0);
}
__device__ __forceinline__ void st16(void* p, u32x4 v) {
    asm volatile("global_store_dwordx4 %0, %1, off sc0 sc1\n\ts_nop 1" :: "v"(p), "v"(v) : "memory");
}
__device__ __forceinline__ float bf2f(u16 x) { return __uint_as_float(((unsigned)x) << 16); }
__device__ __forceinline__ u16 f2bf(float x) { __hip_bfloat16 b = __float2bfloat16(x); return *reinterpret_cast<u16*>(&b); }
__device__ __forceinline__ float rbf(float x) { return bf2f(f2bf(x)); }
__device__ __forceinline__ unsigned pack2(float a, float b) { return (unsigned)f2bf(a) | ((unsigned)f2bf(b) << 16); }
__device__ __forceinline__ float lo16(unsigned w) { return __uint_as_float(w << 16); }
__device__ __forceinline__ float hi16(unsigned w) { return __uint_as_float(w & 0xffff0000u); }

constexpr int K1_A = 16384, K1_B = 16384, K1_STAGE = K1_A + K1_B, K1_NST = 4;
constexpr int K1_LDS = K1_STAGE * K1_NST + 1024;

struct Frag1 { bf16x8 cf[4]; bf16x4 xl[4], xh[4]; };

__global__ __launch_bounds__(512, 2) void k1_gemmA_prep(
    const u16* __restrict__ cr, const u16* __restrict__ ci, const u16* __restrict__ x,
    const u16* __restrict__ kk, const u16* __restrict__ vv,
    const u16* __restrict__ shift_k, const u16* __restrict__ ssm_k, const u16* __restrict__ dr, const u16* __restrict__ di,
    u16* __restrict__ pq, u16* __restrict__ kvT)
{
    extern __shared__ __attribute__((aligned(16))) char smem[];
    const int tid = threadIdx.x, lane = tid & 63;
    const int w8 = __builtin_amdgcn_readfirstlane(tid >> 6);
    const int w = w8 & 3;
    const int bid = blockIdx.x;
    const int xcd = bid & 7, idx = bid >> 3;
    constexpr int PG0 = 2;
    auto touch = [&](int kg, int j) {
        const char* p;
        if (j < 2048) p = (const char*)((j >> 10) ? ci : cr) + (size_t)(256 * xcd + ((j >> 2) & 255)) * 4096 + (4 * kg + (j & 3)) * 128;
        else p = (const char*)x + (size_t)kg * (256 * 1280) + (size_t)(j - 2048) * 128;
        glds4(p, smem + 4 * K1_STAGE + w * 256);
    };
    if (idx >= 20) {
        for (int L = tid; L < 8 * 144; L += 512) touch(L / 144, 144 * idx + L % 144);
    } else if (w8 < 4) {
        for (int L = tid; L < PG0 * 144; L += 256) touch(L / 144, 144 * idx + L % 144);
    }

    if (idx >= 20) {
        const int h0 = xcd * 12 + (idx - 20);
        const int r = tid >> 3, c = tid & 7;
        u32x4 kr[4][4], vr[4];
#pragma unroll
        for (int i = 0; i < 4; ++i) {
            int tile = h0 + 96 * i; tile = tile < 320 ? tile : h0;
            const int t = (tile / 10) * 64 + r, d = (tile % 10) * 64 + 8 * c;
#pragma unroll
            for (int j = 0; j < 4; ++j) { int ts = t + j - 3; kr[i][j] = *reinterpret_cast<const u32x4*>(kk + (size_t)(ts < 0 ? 0 : ts) * D + d); }
            vr[i] = *reinterpret_cast<const u32x4*>(vv + (size_t)t * D + d);
        }
#pragma unroll
        for (int i = 0; i < 4; ++i) {
            const int tile = h0 + 96 * i;
            if (tile < 320) {
                u16* kvs = reinterpret_cast<u16*>(smem) + i * (2 * 64 * 72);
                const int t = (tile / 10) * 64 + r, d = (tile % 10) * 64 + 8 * c;
                u32x4 sk[4];
#pragma unroll
                for (int j = 0; j < 4; ++j) sk[j] = *reinterpret_cast<const u32x4*>(shift_k + d * 4 + 8 * j);
#pragma unroll
                for (int e = 0; e < 8; ++e) {
                    float a = 0.f;
#pragma unroll
                    for (int j = 0; j < 4; ++j) {
                        unsigned kw = (t + j - 3 < 0) ? 0u : kr[i][j][e >> 1];
                        float kf = (e & 1) ? hi16(kw) : lo16(kw);
                        int si = e * 4 + 3 - j;
                        unsigned sw = sk[si >> 3][(si >> 1) & 3];
                        float sf = (si & 1) ? hi16(sw) : lo16(sw);
                        a = rbf(a + rbf(kf * sf));
                    }
                    unsigned vw = vr[i][e >> 1];
                    float vf = (e & 1) ? hi16(vw) : lo16(vw);
                    kvs[(8 * c + e) * 72 + r] = f2bf(a * vf);
                }
            }
        }
        __syncthreads();
#pragma unroll
        for (int i = 0; i < 4; ++i) {
            const int tile = h0 + 96 * i;
            if (tile < 320) {
                const u16* kvs = reinterpret_cast<const u16*>(smem) + i * (2 * 64 * 72);
                const int tt0 = (tile / 10) * 64, d0 = (tile % 10) * 64;
                u32x4 a = *reinterpret_cast<const u32x4*>(kvs + r * 72 + 8 * c);
                st16(kvT + (size_t)(d0 + r) * S + tt0 + 8 * c, a);
            }
        }
        for (int L = (idx - 20) * 512 + tid; L < 15360; L += 12 * 512) {
            const char* p = (L < 6400) ? (const char*)dr + (size_t)L * 128 : (L < 12800) ? (const char*)di + (size_t)(L - 6400) * 128
                          : (const char*)ssm_k + (size_t)xcd * 80 * 4096 + (size_t)(L - 12800) * 128;
            glds4(p, smem + 4 * K1_STAGE + w * 256);
        }
        WAIT_V(0);
        return;
    }

    const int tm = xcd * 4 + idx / 5, tn = idx % 5;
    const int t0 = tm * 64, n0 = tn * 128;

    if (w8 >= 4) {
        const u16* aptr[4];
#pragma unroll
        for (int i = 0; i < 4; ++i) {
            int row = 32 * w + 8 * i + (lane >> 3);
            int ch = (lane & 7) ^ ((row >> 1) & 7);
            aptr[i] = (row < 64 ? cr : ci) + (size_t)(t0 + (row & 63)) * S + ch * 8;
        }
        const u16* bptr[4];
#pragma unroll
        for (int i = 0; i < 4; ++i) {
            int line = (w * 4 + i) * 8 + (lane >> 3);
            int kb = line >> 3, c64 = (line >> 2) & 1, q = line & 3;
            int c = lane & 7;
            int f = ((q >> 1) & 1) | (((kb >> 1) & 1) << 1);
            int cg = ((((c >> 1) ^ f) << 1) | (c & 1));
            bptr[i] = x + (4 * kb + q) * D + n0 + 64 * c64 + 8 * cg;
        }
        auto stage = [&](int kt, int buf) {
            char* sb = smem + buf * K1_STAGE;
#pragma unroll
            for (int i = 0; i < 4; ++i) glds16(aptr[i] + kt * 64, sb + (32 * w + 8 * i) * 128);
#pragma unroll
            for (int i = 0; i < 4; ++i) glds16(bptr[i] + kt * 64 * D, sb + K1_A + (w * 4 + i) * 1024);
        };
        stage(0, 0); stage(1, 1);
        WAIT_V(8); BAR();
        stage(2, 2); stage(3, 3);
        for (int kt4 = 0; kt4 < 28; kt4 += 4) {
            WAIT_V(16); BAR(); stage(kt4 + 4, 0);
            WAIT_V(16); BAR(); stage(kt4 + 5, 1);
            WAIT_V(16); BAR(); stage(kt4 + 6, 2);
            WAIT_V(16); BAR(); stage(kt4 + 7, 3);
        }
        WAIT_V(16); BAR();
        WAIT_V(8); BAR();
        WAIT_V(0); BAR();
    } else {
        const int wm = w & 1, wn = w >> 1;
        f32x4 acc[4][4];
#pragma unroll
        for (int a = 0; a < 4; ++a)
#pragma unroll
            for (int b = 0; b < 4; ++b) acc[a][b] = (f32x4){0.f, 0.f, 0.f, 0.f};
        const int g = lane >> 4;
        const unsigned lds0 = (unsigned)(uintptr_t)smem;
        unsigned aoff[2];
#pragma unroll
        for (int ks = 0; ks < 2; ++ks) {
            int row = 64 * wm + (lane & 15);
            int ch = (4 * ks + g) ^ ((row >> 1) & 7);
            aoff[ks] = lds0 + row * 128 + ch * 16;
        }
        unsigned boff[4];
        {
            int q = (lane & 15) >> 2, p = lane & 3;
            int f = ((q >> 1) & 1) | ((g & 1) << 1);
#pragma unroll
            for (int nt = 0; nt < 4; ++nt)
                boff[nt] = lds0 + K1_A + (2 * g) * 1024 + wn * 512 + q * 128 + ((nt ^ f) * 32) + p * 8;
        }
        auto readfrag = [&](Frag1& f, int buf, int ks) {
            const unsigned bo = buf * K1_STAGE;
            const unsigned a0 = aoff[ks] + bo;
            RD128(f.cf[0], a0, 0); RD128(f.cf[1], a0, 2048); RD128(f.cf[2], a0, 4096); RD128(f.cf[3], a0, 6144);
#pragma unroll
            for (int nt = 0; nt < 4; ++nt) {
                const unsigned b0 = boff[nt] + bo;
                if (ks == 0) { RDTR(f.xl[nt], b0, 0); RDTR(f.xh[nt], b0, 1024); }
                else         { RDTR(f.xl[nt], b0, 8192); RDTR(f.xh[nt], b0, 9216); }
            }
        };
        auto pinfrag = [&](Frag1& f) {
#pragma unroll
            for (int i = 0; i < 4; ++i) { PIN(f.cf[i]); PIN(f.xl[i]); PIN(f.xh[i]); }
        };
        auto mma = [&](Frag1& f) {
#pragma unroll
            for (int nt = 0; nt < 4; ++nt) {
                bf16x8 xf = __builtin_shufflevector(f.xl[nt], f.xh[nt], 0, 1, 2, 3, 4, 5, 6, 7);
#pragma unroll
                for (int mt = 0; mt < 4; ++mt)
                    acc[mt][nt] = __builtin_amdgcn_mfma_f32_16x16x32_bf16(xf, f.cf[mt], acc[mt][nt], 0, 0, 0);
            }
        };
        auto mma_rd = [&](Frag1& fc, Frag1& fn, int buf, int ks) {
            const unsigned bo = buf * K1_STAGE;
            const unsigned a0 = aoff[ks] + bo;
#pragma unroll
            for (int i = 0; i < 16; ++i) {
                const int nt = i >> 2, mt = i & 3;
                bf16x8 xf = __builtin_shufflevector(fc.xl[nt], fc.xh[nt], 0, 1, 2, 3, 4, 5, 6, 7);
                acc[mt][nt] = __builtin_amdgcn_mfma_f32_16x16x32_bf16(xf, fc.cf[mt], acc[mt][nt], 0, 0, 0);
                SCHED();
                if (i < 4) { RD128(fn.cf[i], a0, 2048 * i); }
                else if (i < 12) {
                    const int j = (i - 4) >> 1;
                    const unsigned b0 = boff[j] + bo;
                    if ((i & 1) == 0) { if (ks == 0) RDTR(fn.xl[j], b0, 0); else RDTR(fn.xl[j], b0, 8192); }
                    else { if (ks == 0) RDTR(fn.xh[j], b0, 1024); else RDTR(fn.xh[j], b0, 9216); }
                }
                SCHED();
            }
        };
        Frag1 f0, f1;
        BAR();
        readfrag(f0, 0, 0);
#define KSTEP(b, bn) do { WAIT_L0(); pinfrag(f0); mma_rd(f0, f1, b, 1); BAR(); pinfrag(f1); mma_rd(f1, f0, bn, 0); } while (0)
        for (int kt4 = 0; kt4 < 28; kt4 += 4) {
            { int kg = (kt4 >> 2) + PG0; if (lane < 36 && kg < 8) touch(kg, 144 * idx + 36 * w + lane); }
            KSTEP(0, 1); KSTEP(1, 2); KSTEP(2, 3); KSTEP(3, 0);
        }
        KSTEP(0, 1); KSTEP(1, 2); KSTEP(2, 3);
        WAIT_L0(); pinfrag(f0); mma_rd(f0, f1, 3, 1); WAIT_L0(); pinfrag(f1); mma(f1);
        {
            char* st = smem + w * 9216;
#pragma unroll
            for (int mt = 0; mt < 4; ++mt)
#pragma unroll
                for (int nt = 0; nt < 4; ++nt) {
                    u32x2 o;
                    o[0] = pack2(acc[mt][nt][0], acc[mt][nt][1]);
                    o[1] = pack2(acc[mt][nt][2], acc[mt][nt][3]);
                    *reinterpret_cast<u32x2*>(st + (16 * mt + (lane & 15)) * 144 + 32 * nt + 8 * g) = o;
                }
            WAIT_L0(); __builtin_amdgcn_wave_barrier();
            const int cbase = (wm ? 640 : 0) + n0 + 64 * wn + 8 * (lane & 7);
#pragma unroll
            for (int i = 0; i < 8; ++i) {
                const int r = 8 * i + (lane >> 3);
                u32x4 v = *reinterpret_cast<const u32x4*>(st + r * 144 + 16 * (lane & 7));
                st16(pq + (size_t)(t0 + r) * 1280 + cbase, v);
            }
        }
        WAIT_V(0);
    }
}

constexpr int KVROW = 4640;
constexpr int CSTR = 2240;
constexpr int CV_TOEP = 8 * KVROW;
constexpr int CV_OBUF = CV_TOEP + 8 * 4 * CSTR;
constexpr int OBROW = 2064;
constexpr int K2_A = 8192, K2_B = 16384, K2_STAGE = K2_A + K2_B, K2_NST = 4;
constexpr int K2_LDS = CV_OBUF + 8 * OBROW + 16;

struct HRegs { u32x4 lo[2][3]; u32x2 hi[2][3]; };

__device__ __forceinline__ void load_h(HRegs& h, const u16* __restrict__ hrow, int lane)
{
#pragma unroll
    for (int ph = 0; ph < 2; ++ph)
#pragma unroll
        for (int it = 0; it < 3; ++it) {
            int c = lane + 64 * it;
            int E = 1024 * ph + 1040 - 8 * c;
            h.lo[ph][it] = (u32x4){0u, 0u, 0u, 0u};
            h.hi[ph][it] = (u32x2){0u, 0u};
            if (c < 134 && E - 8 >= 0 && E - 8 < S) h.lo[ph][it] = *reinterpret_cast<const u32x4*>(hrow + E - 8);
            if (c < 134 && E >= 0 && E < S) h.hi[ph][it] = *reinterpret_cast<const u32x2*>(hrow + E);
        }
}

__device__ __forceinline__ void build_toep(const HRegs& h, int ph, char* cw, int lane)
{
#pragma unroll
    for (int it = 0; it < 3; ++it) {
        int c = lane + 64 * it;
        if (c < 134) {
            unsigned W[6] = {h.lo[ph][it][0], h.lo[ph][it][1], h.lo[ph][it][2], h.lo[ph][it][3], h.hi[ph][it][0], h.hi[ph][it][1]};
#pragma unroll
            for (int r = 0; r < 4; ++r) {
                u32x4 o;
#pragma unroll
                for (int m = 0; m < 4; ++m) {
                    if ((r & 1) == 0) { int i = 4 + r / 2 - m; o[m] = __builtin_amdgcn_perm(W[i - 1], W[i], 0x07060100u); }
                    else { int i = (7 + r) / 2 - m; o[m] = __builtin_amdgcn_perm(W[i], W[i], 0x01000302u); }
                }
                *reinterpret_cast<u32x4*>(cw + r * CSTR + 16 * c) = o;
            }
        }
    }
}

struct FragC { bf16x4 ga[4], gb[4]; bf16x8 F[8]; };

template<int ph>
__device__ __forceinline__ void conv_phase(f32x4 (&acc)[8], const unsigned gbase, const unsigned fbase)
{
        constexpr int NF = (ph == 0) ? 8 : 4;
        auto rd = [&](FragC& f, int q) {
            const unsigned ga = gbase + 64 * 8 - 64 * q;
            const unsigned fa_ = fbase + 64 * 8 - 64 * q;
#pragma unroll
            for (int pp = 0; pp < 4; ++pp) { RD64(f.ga[pp], ga, 64 * (24 - 8 * pp)); RD64(f.gb[pp], ga, 64 * (24 - 8 * pp) + 8); }
#pragma unroll
            for (int mm = 0; mm < NF; ++mm) RD128(f.F[mm], fa_, 512 * mm);
        };
        auto pin = [&](FragC& f) {
#pragma unroll
            for (int pp = 0; pp < 4; ++pp) { PIN(f.ga[pp]); PIN(f.gb[pp]); }
#pragma unroll
            for (int mm = 0; mm < NF; ++mm) PIN(f.F[mm]);
        };
        auto mm_rd = [&](FragC& fc, FragC& fn, int q, bool dord) {
            const unsigned ga = gbase + 64 * 8 - 64 * q;
            const unsigned fa_ = fbase + 64 * 8 - 64 * q;
            int ri = 0;
#pragma unroll
            for (int pp = 0; pp < 4; ++pp) {
                const int p = 4 * ph + pp;
                bf16x8 G = __builtin_shufflevector(fc.ga[pp], fc.gb[pp], 0, 1, 2, 3, 4, 5, 6, 7);
#pragma unroll
                for (int n = p; n < 8; ++n) {
                    acc[n] = __builtin_amdgcn_mfma_f32_16x16x32_bf16(G, fc.F[n - p], acc[n], 0, 0, 0);
                    SCHED();
                    if (dord) {
#pragma unroll
                        for (int rep = 0; rep < (ph == 0 ? 1 : 2); ++rep) {
                            if (ri < 8) { if (ri & 1) RD64(fn.gb[ri >> 1], ga, 64 * (24 - 8 * (ri >> 1)) + 8); else RD64(fn.ga[ri >> 1], ga, 64 * (24 - 8 * (ri >> 1))); }
                            else if (ri < 8 + NF) { RD128(fn.F[ri - 8], fa_, 512 * (ri - 8)); }
                            ++ri;
                        }
                    }
                    SCHED();
                }
            }
        };
        FragC fa, fb;
        bf16x8 F8; bf16x4 g64a, g64b;
        rd(fa, 0);
        RD128(F8, fbase, 0);
        RD64(g64a, gbase, 0); RD64(g64b, gbase, 8);
        WAIT_L0(); pin(fa); PIN(F8); PIN(g64a); PIN(g64b);
        mm_rd(fa, fb, 1, true);
        {
#pragma unroll
            for (int pp = 0; pp < 4; ++pp) {
                const int p = 4 * ph + pp;
                bf16x8 G = __builtin_shufflevector(fa.ga[pp], fa.gb[pp], 0, 1, 2, 3, 4, 5, 6, 7);
                if (pp >= 1) acc[p - 1] = __builtin_amdgcn_mfma_f32_16x16x32_bf16(G, F8, acc[p - 1], 0, 0, 0);
            }
            {
                bf16x8 G64 = __builtin_shufflevector(g64a, g64b, 0, 1, 2, 3, 4, 5, 6, 7);
                acc[4 * ph + 3] = __builtin_amdgcn_mfma_f32_16x16x32_bf16(G64, F8, acc[4 * ph + 3], 0, 0, 0);
            }
        }
        SCHED();
        for (int q = 1; q < 7; q += 2) {
            WAIT_L0(); pin(fb); mm_rd(fb, fa, q + 1, true);
            WAIT_L0(); pin(fa); mm_rd(fa, fb, q + 2, true);
        }
        WAIT_L0(); pin(fb); mm_rd(fb, fa, 0, false);
}


__device__ __forceinline__ void conv_store(const char* buf, int rowstride, u16* __restrict__ out, int d0, int t, const u32x4 (&qr)[4])
{
    u32x2 y[8];
#pragma unroll
    for (int e = 0; e < 8; ++e) y[e] = *reinterpret_cast<const u32x2*>(buf + e * rowstride + 2 * t);
#pragma unroll
    for (int r = 0; r < 4; ++r) {
        u32x4 o;
#pragma unroll
        for (int m = 0; m < 4; ++m) {
            unsigned y0 = y[2 * m][r >> 1], y1 = y[2 * m + 1][r >> 1];
            float a = ((r & 1) ? hi16(y0) : lo16(y0)) * lo16(qr[r][m]);
            float b = ((r & 1) ? hi16(y1) : lo16(y1)) * hi16(qr[r][m]);
            o[m] = pack2(a, b);
        }
        *reinterpret_cast<u32x4*>(out + (size_t)(t + r) * 1280 + d0) = o;
    }
}

__device__ __forceinline__ void conv_block(char* smem, int grp, const u16* __restrict__ ssm_k,
    const u16* __restrict__ ssm_D, const u16* __restrict__ kvT, const u16* __restrict__ qq, u16* __restrict__ out)
{
    const int tid = threadIdx.x, lane = tid & 63;
    const int w8 = __builtin_amdgcn_readfirstlane(tid >> 6);
    const int d0 = grp * 8, d = d0 + w8;
    char* cw = smem + CV_TOEP + w8 * (4 * CSTR);
    char* kvrow = smem + w8 * KVROW;
    char* obuf = smem + CV_OBUF;
    const int g = lane >> 4, li = lane & 15;

    unsigned* cnt = reinterpret_cast<unsigned*>(smem + K2_LDS - 16);
    if (tid == 0) *cnt = 0u;
    __syncthreads();
    HRegs h;
    load_h(h, ssm_k + (size_t)d * S, lane);
    u32x4 kvr[4];
#pragma unroll
    for (int i = 0; i < 4; ++i) kvr[i] = *reinterpret_cast<const u32x4*>(kvT + (size_t)d * S + 8 * (lane + 64 * i));
    u32x4 qr[4];
    const float Dd = bf2f(ssm_D[d]);

    f32x4 acc[8];
#pragma unroll
    for (int n = 0; n < 8; ++n) acc[n] = (f32x4){0.f, 0.f, 0.f, 0.f};
    const unsigned gbase = (unsigned)(uintptr_t)cw + (li & 3) * CSTR + 2 * (1040 - 4 * (li >> 2) + 8 * g) - 64 * 32;
    const unsigned fbase = (unsigned)(uintptr_t)kvrow + 2 * (256 + 16 * li + 8 * g) - 64 * 8;

    auto epi = [&](int n0_) {
        u32x2 kv4[4];
#pragma unroll
        for (int n = 0; n < 4; ++n) kv4[n] = *reinterpret_cast<const u32x2*>(kvrow + 2 * (256 + 256 * (n0_ + n) + 16 * li + 4 * g));
#pragma unroll
        for (int n = 0; n < 4; ++n) {
            int t = 256 * (n0_ + n) + 16 * li + 4 * g;
            float o[4];
#pragma unroll
            for (int r = 0; r < 4; ++r) {
                unsigned kw = kv4[n][r >> 1];
                float kvf = (r & 1) ? hi16(kw) : lo16(kw);
                o[r] = rbf(acc[n0_ + n][r]) + rbf(kvf * Dd);
            }
            u32x2 ov; ov[0] = pack2(o[0], o[1]); ov[1] = pack2(o[2], o[3]);
            if (n0_ == 0) *reinterpret_cast<u32x2*>(obuf + w8 * OBROW + 2 * t) = ov;
            else *reinterpret_cast<u32x2*>(kvrow + 2 * (256 + t)) = ov;
        }
    };
    build_toep(h, 0, cw, lane);
    {
        const u32x4 z = (u32x4){0u, 0u, 0u, 0u};
        if (lane < 32) *reinterpret_cast<u32x4*>(kvrow + 16 * lane) = z;
        if (lane < 2) *reinterpret_cast<u32x4*>(kvrow + 2 * (256 + 2048) + 16 * lane) = z;
#pragma unroll
        for (int i = 0; i < 4; ++i) *reinterpret_cast<u32x4*>(kvrow + 512 + 16 * (lane + 64 * i)) = kvr[i];
    }
    WAIT_L0();
    __builtin_amdgcn_wave_barrier();
#pragma unroll
    for (int r = 0; r < 4; ++r) qr[r] = *reinterpret_cast<const u32x4*>(qq + (size_t)(4 * tid + r) * D + d0);
    asm volatile("" ::: "memory");
    conv_phase<0>(acc, gbase, fbase);
    WAIT_L0();
    __builtin_amdgcn_wave_barrier();
    build_toep(h, 1, cw, lane);
    epi(0);
    WAIT_L0();
    __builtin_amdgcn_wave_barrier();
    if (lane == 0) __hip_atomic_fetch_add(cnt, 1u, __ATOMIC_RELAXED, __HIP_MEMORY_SCOPE_WORKGROUP);
    conv_phase<1>(acc, gbase, fbase);
    WAIT_L0();
    __builtin_amdgcn_wave_barrier();
    epi(4);
    if (tid < 256) {
        while (__hip_atomic_load(cnt, __ATOMIC_RELAXED, __HIP_MEMORY_SCOPE_WORKGROUP) < 8u) __builtin_amdgcn_s_sleep(1);
        asm volatile("" ::: "memory");
        conv_store(obuf, OBROW, out, d0, 4 * tid, qr);
    }
    __syncthreads();
    if (tid >= 256) conv_store(smem + 512, KVROW, out, d0, 4 * tid, qr);
}

struct Frag2 { bf16x8 pf[2]; bf16x8 df[4]; };

__global__ __launch_bounds__(512, 2) void k2_gemmB_conv(
    const u16* __restrict__ pq, const u16* __restrict__ dr, const u16* __restrict__ di,
    const u16* __restrict__ ssm_k, const u16* __restrict__ ssm_D,
    const u16* __restrict__ kvT, const u16* __restrict__ qq, u16* __restrict__ out)
{
    extern __shared__ __attribute__((aligned(16))) char smem[];
    const int bid = blockIdx.x;
    const int tid = threadIdx.x, lane = tid & 63;
    const int w8 = __builtin_amdgcn_readfirstlane(tid >> 6);
    const int w = w8 & 3;
    const int xcd = bid & 7, idx = bid >> 3;
    if (idx < 10) {
        conv_block(smem, xcd * 10 + idx, ssm_k, ssm_D, kvT, qq, out);
        return;
    }
    const int gi = idx - 10;
    const int tm = xcd * 4 + gi / 5, tn = gi % 5;
    const int t0 = tm * 64, n0 = tn * 128;
    const int g = lane >> 4;

    if (w8 >= 4) {
        const u16* aptr[2];
#pragma unroll
        for (int i = 0; i < 2; ++i) {
            int row = 16 * w + 8 * i + (lane >> 3);
            int ch = (lane & 7) ^ ((row >> 1) & 7);
            aptr[i] = pq + (size_t)(t0 + row) * 1280 + ch * 8;
        }
        int boffs[4];
#pragma unroll
        for (int i = 0; i < 4; ++i) {
            int row = 32 * w + 8 * i + (lane >> 3);
            int ch = (lane & 7) ^ ((row >> 1) & 7);
            boffs[i] = (n0 + row) * D + ch * 8;
        }
        auto stage = [&](int kt, int buf) {
            char* sb = smem + buf * K2_STAGE;
            const u16* bsrc = (kt < 10) ? (dr + kt * 64) : (di + (kt - 10) * 64);
#pragma unroll
            for (int i = 0; i < 4; ++i) glds16(bsrc + boffs[i], sb + K2_A + (32 * w + 8 * i) * 128);
#pragma unroll
            for (int i = 0; i < 2; ++i) glds16(aptr[i] + kt * 64, sb + (16 * w + 8 * i) * 128);
        };
        stage(0, 0); stage(1, 1);
        WAIT_V(6); BAR();
        stage(2, 2); stage(3, 3);
        for (int kt4 = 0; kt4 < 16; kt4 += 4) {
            WAIT_V(12); BAR(); stage(kt4 + 4, 0);
            WAIT_V(12); BAR(); stage(kt4 + 5, 1);
            WAIT_V(12); BAR(); stage(kt4 + 6, 2);
            WAIT_V(12); BAR(); stage(kt4 + 7, 3);
        }
        WAIT_V(12); BAR();
        WAIT_V(6); BAR();
        WAIT_V(0); BAR();
        return;
    }

    const int wm = w & 1, wn = w >> 1;
    f32x4 acc[2][4], y1[2][4];
#pragma unroll
    for (int a = 0; a < 2; ++a)
#pragma unroll
        for (int b = 0; b < 4; ++b) { acc[a][b] = (f32x4){0.f, 0.f, 0.f, 0.f}; y1[a][b] = acc[a][b]; }
    const unsigned lds0 = (unsigned)(uintptr_t)smem;
    unsigned aoff[2], boff[2];
#pragma unroll
    for (int ks = 0; ks < 2; ++ks) {
        int row = 32 * wm + (lane & 15);
        aoff[ks] = lds0 + row * 128 + (((4 * ks + g) ^ ((row >> 1) & 7)) * 16);
        int r2 = 64 * wn + (lane & 15);
        boff[ks] = lds0 + K2_A + r2 * 128 + (((4 * ks + g) ^ ((r2 >> 1) & 7)) * 16);
    }
    auto readfrag = [&](Frag2& f, int buf, int ks) {
        const unsigned a = aoff[ks] + buf * K2_STAGE, b = boff[ks] + buf * K2_STAGE;
        RD128(f.pf[0], a, 0); RD128(f.pf[1], a, 2048);
#pragma unroll
        for (int nt = 0; nt < 4; ++nt) RD128(f.df[nt], b, 2048 * nt);
    };
    auto pinfrag = [&](Frag2& f) {
        PIN(f.pf[0]); PIN(f.pf[1]);
#pragma unroll
        for (int nt = 0; nt < 4; ++nt) PIN(f.df[nt]);
    };
    auto mma = [&](Frag2& f) {
#pragma unroll
        for (int nt = 0; nt < 4; ++nt)
#pragma unroll
            for (int mt = 0; mt < 2; ++mt)
                acc[mt][nt] = __builtin_amdgcn_mfma_f32_16x16x32_bf16(f.df[nt], f.pf[mt], acc[mt][nt], 0, 0, 0);
    };
    auto mma_rd = [&](Frag2& fc, Frag2& fn, int buf, int ks) {
        const unsigned a = aoff[ks] + buf * K2_STAGE, b = boff[ks] + buf * K2_STAGE;
#pragma unroll
        for (int i = 0; i < 8; ++i) {
            const int nt = i >> 1, mt = i & 1;
            acc[mt][nt] = __builtin_amdgcn_mfma_f32_16x16x32_bf16(fc.df[nt], fc.pf[mt], acc[mt][nt], 0, 0, 0);
            SCHED();
            if (i < 2) { RD128(fn.pf[i], a, 2048 * i); }
            else if (i < 6) { RD128(fn.df[i - 2], b, 2048 * (i - 2)); }
            SCHED();
        }
    };
    auto sw = [&]() {
#pragma unroll
        for (int a = 0; a < 2; ++a)
#pragma unroll
            for (int nt = 0; nt < 4; ++nt) { y1[a][nt] = acc[a][nt]; acc[a][nt] = (f32x4){0.f, 0.f, 0.f, 0.f}; }
    };
    Frag2 f0, f1;
    BAR();
    readfrag(f0, 0, 0);
#define KSTEP2(b, bn) do { WAIT_L0(); pinfrag(f0); mma_rd(f0, f1, b, 1); BAR(); pinfrag(f1); mma_rd(f1, f0, bn, 0); } while (0)
    for (int kt4 = 0; kt4 < 16; kt4 += 4) {
        KSTEP2(0, 1); KSTEP2(1, 2);
        if (kt4 == 8) sw();
        KSTEP2(2, 3); KSTEP2(3, 0);
    }
    KSTEP2(0, 1); KSTEP2(1, 2); KSTEP2(2, 3);
    WAIT_L0(); pinfrag(f0); mma_rd(f0, f1, 3, 1); WAIT_L0(); pinfrag(f1); mma(f1);
    {
        char* st = smem + w * 4608;
#pragma unroll
        for (int mt = 0; mt < 2; ++mt)
#pragma unroll
            for (int nt = 0; nt < 4; ++nt) {
                float o[4];
#pragma unroll
                for (int r = 0; r < 4; ++r) o[r] = rbf(y1[mt][nt][r]) - rbf(acc[mt][nt][r]);
                u32x2 ov; ov[0] = pack2(o[0], o[1]); ov[1] = pack2(o[2], o[3]);
                *reinterpret_cast<u32x2*>(st + (16 * mt + (lane & 15)) * 144 + 32 * nt + 8 * g) = ov;
            }
        WAIT_L0(); __builtin_amdgcn_wave_barrier();
#pragma unroll
        for (int i = 0; i < 4; ++i) {
            const int r = 8 * i + (lane >> 3);
            u32x4 v = *reinterpret_cast<const u32x4*>(st + r * 144 + 16 * (lane & 7));
            st16(out + (size_t)(t0 + 32 * wm + r) * 1280 + 640 + n0 + 64 * wn + 8 * (lane & 7), v);
        }
    }
}

extern "C" void kernel_launch(void* const* d_in, const int* in_sizes, int n_in,
                              void* d_out, int out_size, void* d_ws, size_t ws_size,
                              hipStream_t stream) {
    const u16* q = (const u16*)d_in[0];
    const u16* k = (const u16*)d_in[1];
    const u16* v = (const u16*)d_in[2];
    const u16* shift_k = (const u16*)d_in[3];
    const u16* ssm_k = (const u16*)d_in[4];
    const u16* ssm_D = (const u16*)d_in[5];
    const u16* x = (const u16*)d_in[6];
    const u16* cr = (const u16*)d_in[7];
    const u16* ci = (const u16*)d_in[8];
    const u16* dr = (const u16*)d_in[9];
    const u16* di = (const u16*)d_in[10];
    u16* out = (u16*)d_out;
    u16* pq = (u16*)d_ws;
    u16* kvT = pq + (size_t)S * 1280 + 65536;
    (void)hipFuncSetAttribute((const void*)k1_gemmA_prep, hipFuncAttributeMaxDynamicSharedMemorySize, K1_LDS);
    (void)hipFuncSetAttribute((const void*)k2_gemmB_conv, hipFuncAttributeMaxDynamicSharedMemorySize, K2_LDS);
    k1_gemmA_prep<<<256, 512, K1_LDS, stream>>>(cr, ci, x, k, v, shift_k, ssm_k, dr, di, pq, kvT);
    k2_gemmB_conv<<<240, 512, K2_LDS, stream>>>(pq, dr, di, ssm_k, ssm_D, kvT, q, out);
}
```
